```python
import torch

def _softmax(x, axis=-1):
    return torch.softmax(x, dim=axis)
def _gelu(x):
    return torch.nn.functional.gelu(x, approximate="tanh")
def _silu(x):
    return torch.nn.functional.silu(x)
def _sigmoid(x):
    return torch.sigmoid(x)
def _layernorm(x, axis=-1, eps=1e-5):
    m = x.mean(dim=axis, keepdim=True)
    v = x.var(dim=axis, keepdim=True, unbiased=False)
    return (x - m) / torch.sqrt(v + eps)
def _scatter_add(tgt, idx, src, axis=0):
    return tgt.clone().index_add_(axis, idx, src)
def _segment_sum(src, seg, n):
    out = torch.zeros((n,) + src.shape[1:], dtype=src.dtype)
    return out.index_add_(0, seg, src)
def _pad(x, pw):
    flat = tuple(v for pair in reversed(pw) for v in pair)
    return torch.nn.functional.pad(x, flat)

kernel_name = 'recurrent_scan__mha_block_cached__moe_arch_mod__decode'

def setup_inputs():

    _w = (lambda a: (lambda t: t.to(torch.bfloat16) if t.is_floating_point() else t)(torch.as_tensor(a)))
    def _A_setup():
        B, Sq, D = 2, 1, 640
        H, Sk, Dh = 32, 8192, 192
        return {
            "x": _w((lambda *s: torch.randn(*s, dtype=torch.bfloat16) * 0.05)(B, Sq, D)),
            "k_cache": _w((lambda *s: torch.randn(*s, dtype=torch.bfloat16) * 1.6)(B, H, Sk, Dh)),
            "v_cache": _w((lambda *s: torch.randn(*s, dtype=torch.bfloat16) * 0.2)(B, H, Sk, Dh)),
            "wq": _w((lambda *s: torch.randn(*s, dtype=torch.bfloat16) * 1.6)(D, H * Dh) * (D ** -0.5)),
            "wo": _w((lambda *s: torch.randn(*s, dtype=torch.bfloat16) * 0.2)(H * Dh, D) * ((H * Dh) ** -0.5)),
        }
    _A_in = _A_setup()

    def _B_setup():
        B, S, D, F, C = 2, 1, 640, 1536, 1
        return {
            "x": _w((lambda *s: torch.randn(*s, dtype=torch.bfloat16) * 0.05)(B, S, D)),
            "w_r": _w((lambda *s: torch.randn(*s, dtype=torch.bfloat16) * 0.1)(D)),
            "w_gate": _w((lambda *s: torch.randn(*s, dtype=torch.bfloat16) * 0.1)(D, F)),
            "w_up": _w((lambda *s: torch.randn(*s, dtype=torch.bfloat16) * 0.1)(D, F)),
            "w_down": _w((lambda *s: torch.randn(*s, dtype=torch.bfloat16) * 0.1)(F, D)),
        }
    _B_in = _B_setup()

    return {
        "A_x": _A_in["x"],
        "A_k_cache": _A_in["k_cache"],
        "A_v_cache": _A_in["v_cache"],
        "A_wq": _A_in["wq"],
        "A_wo": _A_in["wo"],
        "B_x": _B_in["x"],
        "B_w_r": _B_in["w_r"],
        "B_w_gate": _B_in["w_gate"],
        "B_w_up": _B_in["w_up"],
        "B_w_down": _B_in["w_down"],
    }

def reference(A_x, A_k_cache, A_v_cache, A_wq, A_wo, B_x, B_w_r, B_w_gate, B_w_up, B_w_down):
    def _A_ref(x, k_cache, v_cache, wq, wo):
        B, Sq, D = x.shape
        H, Dh = 32, 192
        q = (lambda x, *d: x.permute(*d))((x @ wq).reshape(B, Sq, H, Dh), 0, 2, 1, 3)
        scores = torch.einsum("bhqd,bhkd->bhqk", q, k_cache) * (Dh ** -0.5)
        p = _softmax(scores, axis=-1)
        o = torch.einsum("bhqk,bhkd->bhqd", p, v_cache)
        return (lambda x, *d: x.permute(*d))(o, 0, 2, 1, 3).reshape(B, Sq, H * Dh) @ wo

    def _B_ref(x, w_r, w_gate, w_up, w_down):
        B, S, D = x.shape
        C = 1
        r = x @ w_r
        idx = torch.argsort(-r, axis=-1)[:, :C]
        idx_e = (lambda x, n, axis: torch.repeat_interleave(x, n, dim=axis))(idx.reshape(B, C, 1), D, axis=2)
        x_sel = (lambda x, i, axis: torch.take_along_dim(x, i, dim=axis))(x, idx_e, axis=1)
        g = _silu(x_sel @ w_gate)
        u = x_sel @ w_up
        y = (g * u) @ w_down
        r_sel = _sigmoid((lambda x, i, axis: torch.take_along_dim(x, i, dim=axis))(r, idx, axis=1))
        y = y * r_sel.reshape(B, C, 1)
        b_off = (torch.arange(B) * S).reshape(B, 1)
        flat_idx = (idx + b_off).reshape(B * C)
        zero = (x * 0.0).reshape(B * S, D)
        upd = _scatter_add(zero, flat_idx, y.reshape(B * C, D), axis=0)
        return x + upd.reshape(B, S, D)

    x = A_x
    xn = _layernorm(x, axis=-1)
    s = _A_ref(xn, A_k_cache, A_v_cache, A_wq, A_wo)
    g = _silu(_B_ref(xn, B_w_r, B_w_gate, B_w_up, B_w_down))
    return x + s * g
```

```cpp
#include <hip/hip_runtime.h>
#include <hip/hip_bf16.h>
#include <stdint.h>

typedef unsigned u32x4 __attribute__((ext_vector_type(4)));
typedef __bf16 bf16x2 __attribute__((ext_vector_type(2)));
typedef unsigned short u16;

constexpr int B = 2, D = 640, H = 32, SK = 8192, DH = 192, F = 1536;
constexpr int NSPLIT = 4, KEYS = SK / NSPLIT;

__device__ __forceinline__ float bf2f(u16 v) { return __uint_as_float((unsigned)v << 16); }
__device__ __forceinline__ u16 f2bf(float f) { return __builtin_bit_cast(u16, (__bf16)f); }
__device__ __forceinline__ float rbf(float f) { return bf2f(f2bf(f)); }
__device__ __forceinline__ float dot2(unsigned a, unsigned b, float c) {
  return __builtin_amdgcn_fdot2_f32_bf16(__builtin_bit_cast(bf16x2, a), __builtin_bit_cast(bf16x2, b), c, false);
}
template <int CTRL> __device__ __forceinline__ float dpp(float x) {
  return __builtin_bit_cast(float, __builtin_amdgcn_mov_dpp(__builtin_bit_cast(int, x), CTRL, 0xf, 0xf, true));
}
__device__ __forceinline__ float wave_sum(float v) {
  for (int o = 32; o > 0; o >>= 1) v += __shfl_xor(v, o);
  return v;
}

__device__ void ln_to_lds(const u16* x, float* xn, int tid) {
  int w = tid >> 6, lane = tid & 63;
  if (w < 2) {
    float v[10]; float s = 0;
    for (int i = 0; i < 10; i++) { v[i] = bf2f(x[w * D + lane + 64 * i]); s += v[i]; }
    s = wave_sum(s); float mean = s / D;
    float q = 0; for (int i = 0; i < 10; i++) { float d = v[i] - mean; q += d * d; }
    q = wave_sum(q); float var = rbf(q / D); float mb = rbf(mean);
    float den = rbf(sqrtf(rbf(var + 1e-5f)));
    for (int i = 0; i < 10; i++) xn[w * D + lane + 64 * i] = rbf(rbf(v[i] - mb) / den);
  }
  __syncthreads();
}

__global__ void __launch_bounds__(256) k_pre(const u16* x, const u16* wq, const u16* wg, const u16* wu, u16* q, float* hbuf) {
  __shared__ float xn[2 * D];
  __shared__ float red[4][4][64];
  int tid = threadIdx.x, w = tid >> 6, lane = tid & 63;
  ln_to_lds(x, xn, tid);
  int blk = blockIdx.x;
  if (blk < 96) {
    int col = blk * 64 + lane;
    float a0 = 0, a1 = 0;
    for (int k = w * 160; k < w * 160 + 160; k++) { float wv = bf2f(wq[(size_t)k * (H * DH) + col]); a0 += xn[k] * wv; a1 += xn[D + k] * wv; }
    red[w][0][lane] = a0; red[w][1][lane] = a1;
    __syncthreads();
    if (w < 2) { float s = red[0][w][lane] + red[1][w][lane] + red[2][w][lane] + red[3][w][lane]; q[w * (H * DH) + col] = f2bf(s); }
  } else {
    int col = (blk - 96) * 64 + lane;
    float g0 = 0, g1 = 0, u0 = 0, u1 = 0;
    for (int k = w * 160; k < w * 160 + 160; k++) { float a = bf2f(wg[(size_t)k * F + col]), b = bf2f(wu[(size_t)k * F + col]);
      g0 += xn[k] * a; g1 += xn[D + k] * a; u0 += xn[k] * b; u1 += xn[D + k] * b; }
    red[w][0][lane] = g0; red[w][1][lane] = g1; red[w][2][lane] = u0; red[w][3][lane] = u1;
    __syncthreads();
    if (w < 2) {
      float g = rbf(red[0][w][lane] + red[1][w][lane] + red[2][w][lane] + red[3][w][lane]);
      float u = rbf(red[0][2 + w][lane] + red[1][2 + w][lane] + red[2][2 + w][lane] + red[3][2 + w][lane]);
      float sg = rbf(g / (1.f + __expf(-g)));
      hbuf[w * F + col] = rbf(sg * u);
    }
  }
}

constexpr int RING = 4;
__global__ void __launch_bounds__(512, 2) k_attn(const u16* __restrict__ Kc, const u16* __restrict__ Vc, const u16* __restrict__ q, float* __restrict__ part) {
  __shared__ float sm[8][200];
  const int blk = blockIdx.x, bh = blk >> 2, sp = blk & 3;
  const int tid = threadIdx.x, lane = tid & 63, wave = __builtin_amdgcn_readfirstlane(tid >> 6), r = lane >> 3, c = lane & 7;
  const size_t base = ((size_t)bh * SK + (size_t)sp * KEYS + wave * 8) * DH * 2;
  const char* kb = (const char*)Kc + base; const char* vb = (const char*)Vc + base;
  const unsigned loff = r * 384 + c * 16;
  u32x4 qf[3];
  { const char* qp = (const char*)q + (size_t)bh * DH * 2 + c * 16;
    for (int j = 0; j < 3; j++) qf[j] = *(const u32x4*)(qp + 128 * j); }
  u32x4 kr[RING][3], vr[RING][3];
  constexpr int NST = KEYS / 64;
  constexpr int STEP = 64 * 384;
#pragma unroll
  for (int s = 0; s < RING; s++) {
#pragma unroll
    for (int j = 0; j < 3; j++) kr[s][j] = __builtin_nontemporal_load((const u32x4*)(kb + (size_t)s * STEP + loff + 128 * j));
#pragma unroll
    for (int j = 0; j < 3; j++) vr[s][j] = __builtin_nontemporal_load((const u32x4*)(vb + (size_t)s * STEP + loff + 128 * j));
  }
  float m = -INFINITY, l = 0.f, acc[24];
#pragma unroll
  for (int i = 0; i < 24; i++) acc[i] = 0.f;
  const float scale = 0.07216878364870322f, LOG2E = 1.4426950408889634f;
  for (int it = 0; it < NST; it += RING) {
    const bool more = it + RING < NST;
#pragma unroll
    for (int s = 0; s < RING; s++) {
      float d0 = 0.f, d1 = 0.f, d2 = 0.f;
#pragma unroll
      for (int k = 0; k < 4; k++) { d0 = dot2(kr[s][0][k], qf[0][k], d0); d1 = dot2(kr[s][1][k], qf[1][k], d1); d2 = dot2(kr[s][2][k], qf[2][k], d2); }
      if (more) {
#pragma unroll
        for (int j = 0; j < 3; j++) kr[s][j] = __builtin_nontemporal_load((const u32x4*)(kb + (size_t)(it + RING + s) * STEP + loff + 128 * j));
      }
      float d = d0 + d1 + d2;
      d += dpp<0xB1>(d); d += dpp<0x4E>(d); d += dpp<0x141>(d);
      float t = rbf(rbf(d) * scale) * LOG2E;
      float mn = fmaxf(m, t);
      float alpha = __builtin_amdgcn_exp2f(m - mn), p = __builtin_amdgcn_exp2f(t - mn);
      m = mn; l = l * alpha + p;
#pragma unroll
      for (int j = 0; j < 3; j++)
#pragma unroll
        for (int k = 0; k < 4; k++) {
          unsigned w = vr[s][j][k];
          acc[j * 8 + 2 * k] = acc[j * 8 + 2 * k] * alpha + p * __uint_as_float(w << 16);
          acc[j * 8 + 2 * k + 1] = acc[j * 8 + 2 * k + 1] * alpha + p * __uint_as_float(w & 0xffff0000u);
        }
      if (more) {
#pragma unroll
        for (int j = 0; j < 3; j++) vr[s][j] = __builtin_nontemporal_load((const u32x4*)(vb + (size_t)(it + RING + s) * STEP + loff + 128 * j));
      }
    }
  }
  float M = m; M = fmaxf(M, __shfl_xor(M, 8)); M = fmaxf(M, __shfl_xor(M, 16)); M = fmaxf(M, __shfl_xor(M, 32));
  float f = __builtin_amdgcn_exp2f(m - M);
  l *= f; l += __shfl_xor(l, 8); l += __shfl_xor(l, 16); l += __shfl_xor(l, 32);
#pragma unroll
  for (int i = 0; i < 24; i++) { float a = acc[i] * f; a += __shfl_xor(a, 8); a += __shfl_xor(a, 16); a += __shfl_xor(a, 32); acc[i] = a; }
  if (r == 0) {
#pragma unroll
    for (int j = 0; j < 3; j++)
#pragma unroll
      for (int k = 0; k < 8; k++) sm[wave][j * 64 + c * 8 + k] = acc[j * 8 + k];
    if (c == 0) { sm[wave][192] = M; sm[wave][193] = l; }
  }
  __syncthreads();
  if (tid < 192) {
    float MM = -INFINITY;
    for (int w = 0; w < 8; w++) MM = fmaxf(MM, sm[w][192]);
    float L = 0, o = 0;
    for (int w = 0; w < 8; w++) { float e = __builtin_amdgcn_exp2f(sm[w][192] - MM); L += e * sm[w][193]; o += e * sm[w][tid]; }
    part[blk * 256 + tid] = o;
    if (tid == 0) { part[blk * 256 + 192] = MM; part[blk * 256 + 193] = L; }
  }
}

__global__ void __launch_bounds__(1024) k_post(const u16* x, const float* part, const u16* wo, const float* hbuf, const u16* wd, const u16* wr, u16* out) {
  __shared__ float xn[2 * D];
  __shared__ float o[H * DH];
  __shared__ float red[16][2][64];
  __shared__ float rs;
  int tid = threadIdx.x, w = tid >> 6, lane = tid & 63, b = blockIdx.y;
  ln_to_lds(x, xn, tid);
  for (int i = tid; i < H * DH; i += 1024) {
    int h = i / DH, d = i % DH; const float* p = part + (size_t)((b * H + h) * 4) * 256;
    float MM = fmaxf(fmaxf(p[192], p[256 + 192]), fmaxf(p[512 + 192], p[768 + 192]));
    float L = 0, a = 0;
    for (int s = 0; s < 4; s++) { float e = __builtin_amdgcn_exp2f(p[s * 256 + 192] - MM); L += e * p[s * 256 + 193]; a += e * p[s * 256 + d]; }
    o[i] = rbf(a / L);
  }
  if (w == 0) { float s = 0; for (int i = lane; i < D; i += 64) s += xn[b * D + i] * bf2f(wr[i]); s = wave_sum(s); if (lane == 0) { float rr = rbf(s); rs = rbf(1.f / (1.f + __expf(-rr))); } }
  __syncthreads();
  int col = blockIdx.x * 64 + lane;
  float s = 0, y = 0;
  for (int k = w * 384; k < w * 384 + 384; k++) s += o[k] * bf2f(wo[(size_t)k * D + col]);
  for (int k = w * 96; k < w * 96 + 96; k++) y += hbuf[b * F + k] * bf2f(wd[(size_t)k * D + col]);
  red[w][0][lane] = s; red[w][1][lane] = y;
  __syncthreads();
  if (w == 0) {
    float S = 0, Y = 0; for (int i = 0; i < 16; i++) { S += red[i][0][lane]; Y += red[i][1][lane]; }
    S = rbf(S); Y = rbf(rbf(Y) * rs);
    float z = rbf(xn[b * D + col] + Y);
    float g = rbf(z / (1.f + __expf(-z)));
    out[b * D + col] = f2bf(bf2f(x[b * D + col]) + rbf(S * g));
  }
}

extern "C" void kernel_launch(void* const* d_in, const int* in_sizes, int n_in, void* d_out, int out_size, void* d_ws, size_t ws_size, hipStream_t stream) {
  const u16* x = (const u16*)d_in[0]; const u16* Kc = (const u16*)d_in[1]; const u16* Vc = (const u16*)d_in[2];
  const u16* wq = (const u16*)d_in[3]; const u16* wo = (const u16*)d_in[4];
  const u16* wr = (const u16*)d_in[6]; const u16* wg = (const u16*)d_in[7]; const u16* wu = (const u16*)d_in[8]; const u16* wd = (const u16*)d_in[9];
  char* ws = (char*)d_ws;
  u16* q = (u16*)ws; float* hbuf = (float*)(ws + 32768); float* part = (float*)(ws + 65536);
  k_pre<<<120, 256, 0, stream>>>(x, wq, wg, wu, q, hbuf);
  k_attn<<<256, 512, 0, stream>>>(Kc, Vc, q, part);
  k_post<<<dim3(10, 2), 1024, 0, stream>>>(x, part, wo, hbuf, wd, wr, (u16*)d_out);
}
```

```cpp
#include <hip/hip_runtime.h>
#include <hip/hip_bf16.h>
#include <stdint.h>

typedef unsigned u32x4 __attribute__((ext_vector_type(4)));
typedef __bf16 bf16x2 __attribute__((ext_vector_type(2)));
typedef unsigned short u16;

constexpr int B = 2, D = 640, H = 32, SK = 8192, DH = 192, F = 1536;
constexpr int NSPLIT = 4, KEYS = SK / NSPLIT;

__device__ __forceinline__ float bf2f(u16 v) { return __uint_as_float((unsigned)v << 16); }
__device__ __forceinline__ u16 f2bf(float f) { return __builtin_bit_cast(u16, (__bf16)f); }
__device__ __forceinline__ float rbf(float f) { return bf2f(f2bf(f)); }
__device__ __forceinline__ float dot2(unsigned a, unsigned b, float c) {
  return __builtin_amdgcn_fdot2_f32_bf16(__builtin_bit_cast(bf16x2, a), __builtin_bit_cast(bf16x2, b), c, false);
}
template <int CTRL> __device__ __forceinline__ float dpp(float x) {
  return __builtin_bit_cast(float, __builtin_amdgcn_mov_dpp(__builtin_bit_cast(int, x), CTRL, 0xf, 0xf, true));
}
__device__ __forceinline__ float wave_sum(float v) {
  for (int o = 32; o > 0; o >>= 1) v += __shfl_xor(v, o);
  return v;
}

__device__ void ln_to_lds(const u16* x, float* xn, int tid) {
  int w = tid >> 6, lane = tid & 63;
  if (w < 2) {
    float v[10]; float s = 0;
    for (int i = 0; i < 10; i++) { v[i] = bf2f(x[w * D + lane + 64 * i]); s += v[i]; }
    s = wave_sum(s); float mean = s / D;
    float q = 0; for (int i = 0; i < 10; i++) { float d = v[i] - mean; q += d * d; }
    q = wave_sum(q); float var = rbf(q / D); float mb = rbf(mean);
    float den = rbf(sqrtf(rbf(var + 1e-5f)));
    for (int i = 0; i < 10; i++) xn[w * D + lane + 64 * i] = rbf(rbf(v[i] - mb) / den);
  }
  __syncthreads();
}

__global__ void __launch_bounds__(256) k_pre(const u16* x, const u16* wq, const u16* wg, const u16* wu, u16* q, float* hbuf) {
  __shared__ float xn[2 * D];
  __shared__ float red[4][4][64];
  int tid = threadIdx.x, w = tid >> 6, lane = tid & 63;
  ln_to_lds(x, xn, tid);
  int blk = blockIdx.x;
  if (blk < 96) {
    int col = blk * 64 + lane;
    float a0 = 0, a1 = 0;
    for (int k = w * 160; k < w * 160 + 160; k++) { float wv = bf2f(wq[(size_t)k * (H * DH) + col]); a0 += xn[k] * wv; a1 += xn[D + k] * wv; }
    red[w][0][lane] = a0; red[w][1][lane] = a1;
    __syncthreads();
    if (w < 2) { float s = red[0][w][lane] + red[1][w][lane] + red[2][w][lane] + red[3][w][lane]; q[w * (H * DH) + col] = f2bf(s); }
  } else {
    int col = (blk - 96) * 64 + lane;
    float g0 = 0, g1 = 0, u0 = 0, u1 = 0;
    for (int k = w * 160; k < w * 160 + 160; k++) { float a = bf2f(wg[(size_t)k * F + col]), b = bf2f(wu[(size_t)k * F + col]);
      g0 += xn[k] * a; g1 += xn[D + k] * a; u0 += xn[k] * b; u1 += xn[D + k] * b; }
    red[w][0][lane] = g0; red[w][1][lane] = g1; red[w][2][lane] = u0; red[w][3][lane] = u1;
    __syncthreads();
    if (w < 2) {
      float g = rbf(red[0][w][lane] + red[1][w][lane] + red[2][w][lane] + red[3][w][lane]);
      float u = rbf(red[0][2 + w][lane] + red[1][2 + w][lane] + red[2][2 + w][lane] + red[3][2 + w][lane]);
      float sg = rbf(g / (1.f + __expf(-g)));
      hbuf[w * F + col] = rbf(sg * u);
    }
  }
}

#define AUXV 2
constexpr int RING = 2;
constexpr unsigned KVB = 2u * 32u * 8192u * 192u * 2u;
__global__ void __launch_bounds__(512, 2) k_attn(const u16* __restrict__ Kc, const u16* __restrict__ Vc, const u16* __restrict__ q, float* __restrict__ part) {
  __shared__ float sm[8][200];
  const int blk = blockIdx.x, bh = blk >> 2, sp = blk & 3;
  const int tid = threadIdx.x, lane = tid & 63, wave = __builtin_amdgcn_readfirstlane(tid >> 6), r = lane >> 3, c = lane & 7;
  constexpr int NST = KEYS / 64;
  constexpr int STEP = 3072;
  __amdgpu_buffer_rsrc_t rk = __builtin_amdgcn_make_buffer_rsrc((void*)Kc, 0, KVB, 0x00020000);
  __amdgpu_buffer_rsrc_t rv = __builtin_amdgcn_make_buffer_rsrc((void*)Vc, 0, KVB, 0x00020000);
  unsigned so = (unsigned)blk * KEYS * 384u + wave * (unsigned)(NST * 3072);
  const unsigned loff = r * 384 + c * 16;
  u32x4 qf[3];
  { const char* qp = (const char*)q + (size_t)bh * DH * 2 + c * 16;
    for (int j = 0; j < 3; j++) qf[j] = *(const u32x4*)(qp + 128 * j); }
  u32x4 kr[RING][3], vr[RING][3];
#pragma unroll
  for (int s = 0; s < RING; s++) {
#pragma unroll
    for (int j = 0; j < 3; j++) kr[s][j] = __builtin_amdgcn_raw_buffer_load_b128(rk, loff + 128 * j, so + s * STEP, AUXV);
#pragma unroll
    for (int j = 0; j < 3; j++) vr[s][j] = __builtin_amdgcn_raw_buffer_load_b128(rv, loff + 128 * j, so + s * STEP, AUXV);
  }
  __builtin_amdgcn_sched_barrier(0);
  float m = -INFINITY, l = 0.f, acc[24];
#pragma unroll
  for (int i = 0; i < 24; i++) acc[i] = 0.f;
  const float scale = 0.07216878364870322f, LOG2E = 1.4426950408889634f;
#define STEP_BODY(s, MORE) { \
      float d0 = 0.f, d1 = 0.f, d2 = 0.f; \
      _Pragma("unroll") for (int k = 0; k < 4; k++) { d0 = dot2(kr[s][0][k], qf[0][k], d0); d1 = dot2(kr[s][1][k], qf[1][k], d1); d2 = dot2(kr[s][2][k], qf[2][k], d2); } \
      __builtin_amdgcn_sched_barrier(0); \
      if (MORE) { _Pragma("unroll") for (int j = 0; j < 3; j++) kr[s][j] = __builtin_amdgcn_raw_buffer_load_b128(rk, loff + 128 * j, so + s * STEP, AUXV); } \
      __builtin_amdgcn_sched_barrier(0); \
      float d = d0 + d1 + d2; \
      d += dpp<0xB1>(d); d += dpp<0x4E>(d); d += dpp<0x141>(d); \
      float t = rbf(rbf(d) * scale) * LOG2E; \
      float mn = fmaxf(m, t); \
      float alpha = __builtin_amdgcn_exp2f(m - mn), p = __builtin_amdgcn_exp2f(t - mn); \
      m = mn; l = l * alpha + p; \
      _Pragma("unroll") for (int j = 0; j < 3; j++) \
        _Pragma("unroll") for (int k = 0; k < 4; k++) { \
          unsigned w = vr[s][j][k]; \
          acc[j * 8 + 2 * k] = acc[j * 8 + 2 * k] * alpha + p * __uint_as_float(w << 16); \
          acc[j * 8 + 2 * k + 1] = acc[j * 8 + 2 * k + 1] * alpha + p * __uint_as_float(w & 0xffff0000u); \
        } \
      __builtin_amdgcn_sched_barrier(0); \
      if (MORE) { _Pragma("unroll") for (int j = 0; j < 3; j++) vr[s][j] = __builtin_amdgcn_raw_buffer_load_b128(rv, loff + 128 * j, so + s * STEP, AUXV); } \
      __builtin_amdgcn_sched_barrier(0); \
  }
#pragma unroll 1
  for (int it = 0; it < NST - RING; it += RING) {
    so += RING * STEP;
#pragma unroll
    for (int s = 0; s < RING; s++) STEP_BODY(s, true)
  }
#pragma unroll
  for (int s = 0; s < RING; s++) STEP_BODY(s, false)
  float M = m; M = fmaxf(M, __shfl_xor(M, 8)); M = fmaxf(M, __shfl_xor(M, 16)); M = fmaxf(M, __shfl_xor(M, 32));
  float f = __builtin_amdgcn_exp2f(m - M);
  l *= f; l += __shfl_xor(l, 8); l += __shfl_xor(l, 16); l += __shfl_xor(l, 32);
#pragma unroll
  for (int i = 0; i < 24; i++) { float a = acc[i] * f; a += __shfl_xor(a, 8); a += __shfl_xor(a, 16); a += __shfl_xor(a, 32); acc[i] = a; }
  if (r == 0) {
#pragma unroll
    for (int j = 0; j < 3; j++)
#pragma unroll
      for (int k = 0; k < 8; k++) sm[wave][j * 64 + c * 8 + k] = acc[j * 8 + k];
    if (c == 0) { sm[wave][192] = M; sm[wave][193] = l; }
  }
  __syncthreads();
  if (tid < 192) {
    float MM = -INFINITY;
    for (int w = 0; w < 8; w++) MM = fmaxf(MM, sm[w][192]);
    float L = 0, o = 0;
    for (int w = 0; w < 8; w++) { float e = __builtin_amdgcn_exp2f(sm[w][192] - MM); L += e * sm[w][193]; o += e * sm[w][tid]; }
    part[blk * 256 + tid] = o;
    if (tid == 0) { part[blk * 256 + 192] = MM; part[blk * 256 + 193] = L; }
  }
}

__global__ void __launch_bounds__(1024) k_post(const u16* x, const float* part, const u16* wo, const float* hbuf, const u16* wd, const u16* wr, u16* out) {
  __shared__ float xn[2 * D];
  __shared__ float o[H * DH];
  __shared__ float red[16][2][64];
  __shared__ float rs;
  int tid = threadIdx.x, w = tid >> 6, lane = tid & 63, b = blockIdx.y;
  ln_to_lds(x, xn, tid);
  for (int i = tid; i < H * DH; i += 1024) {
    int h = i / DH, d = i % DH; const float* p = part + (size_t)((b * H + h) * 4) * 256;
    float MM = fmaxf(fmaxf(p[192], p[256 + 192]), fmaxf(p[512 + 192], p[768 + 192]));
    float L = 0, a = 0;
    for (int s = 0; s < 4; s++) { float e = __builtin_amdgcn_exp2f(p[s * 256 + 192] - MM); L += e * p[s * 256 + 193]; a += e * p[s * 256 + d]; }
    o[i] = rbf(a / L);
  }
  if (w == 0) { float s = 0; for (int i = lane; i < D; i += 64) s += xn[b * D + i] * bf2f(wr[i]); s = wave_sum(s); if (lane == 0) { float rr = rbf(s); rs = rbf(1.f / (1.f + __expf(-rr))); } }
  __syncthreads();
  int col = blockIdx.x * 64 + lane;
  float s = 0, y = 0;
  for (int k = w * 384; k < w * 384 + 384; k++) s += o[k] * bf2f(wo[(size_t)k * D + col]);
  for (int k = w * 96; k < w * 96 + 96; k++) y += hbuf[b * F + k] * bf2f(wd[(size_t)k * D + col]);
  red[w][0][lane] = s; red[w][1][lane] = y;
  __syncthreads();
  if (w == 0) {
    float S = 0, Y = 0; for (int i = 0; i < 16; i++) { S += red[i][0][lane]; Y += red[i][1][lane]; }
    S = rbf(S); Y = rbf(rbf(Y) * rs);
    float z = rbf(xn[b * D + col] + Y);
    float g = rbf(z / (1.f + __expf(-z)));
    out[b * D + col] = f2bf(bf2f(x[b * D + col]) + rbf(S * g));
  }
}

extern "C" void kernel_launch(void* const* d_in, const int* in_sizes, int n_in, void* d_out, int out_size, void* d_ws, size_t ws_size, hipStream_t stream) {
  const u16* x = (const u16*)d_in[0]; const u16* Kc = (const u16*)d_in[1]; const u16* Vc = (const u16*)d_in[2];
  const u16* wq = (const u16*)d_in[3]; const u16* wo = (const u16*)d_in[4];
  const u16* wr = (const u16*)d_in[6]; const u16* wg = (const u16*)d_in[7]; const u16* wu = (const u16*)d_in[8]; const u16* wd = (const u16*)d_in[9];
  char* ws = (char*)d_ws;
  u16* q = (u16*)ws; float* hbuf = (float*)(ws + 32768); float* part = (float*)(ws + 65536);
  k_pre<<<120, 256, 0, stream>>>(x, wq, wg, wu, q, hbuf);
  k_attn<<<256, 512, 0, stream>>>(Kc, Vc, q, part);
  k_post<<<dim3(10, 2), 1024, 0, stream>>>(x, part, wo, hbuf, wd, wr, (u16*)d_out);
}
```

```cpp
#include <hip/hip_runtime.h>
#include <stdint.h>

typedef unsigned u32; typedef unsigned long long u64; typedef unsigned short u16;
typedef u32 u32x4 __attribute__((ext_vector_type(4)));
typedef u32 u32x2 __attribute__((ext_vector_type(2)));
typedef int i32x4 __attribute__((ext_vector_type(4)));
typedef float f32x2 __attribute__((ext_vector_type(2)));
typedef float f32x4 __attribute__((ext_vector_type(4)));
typedef short s16x4 __attribute__((ext_vector_type(4)));
typedef short s16x8 __attribute__((ext_vector_type(8)));
typedef __bf16 bf16x2 __attribute__((ext_vector_type(2)));
typedef __bf16 bf16x8 __attribute__((ext_vector_type(8)));
#define GAS __attribute__((address_space(1)))
#define LAS __attribute__((address_space(3)))
#define RLX_AGENT __ATOMIC_RELAXED, __HIP_MEMORY_SCOPE_AGENT

#define WO_LATE 0

constexpr int D = 640, H = 32, SK = 8192, DH = 192, F = 1536;
constexpr u32 KVB = 2u * 32u * 8192u * 192u * 2u;
constexpr size_t WS_G0 = 4096, WS_G1 = WS_G0 + 256 * 576 * 8, WS_G2 = WS_G1 + 256 * 256 * 8, WS_END = WS_G2 + 256 * 320 * 8;
constexpr int L_WQ = 0, L_GU = L_WQ + 3 * 80 * 160, L_WO = L_GU + 960 * 16, L_PART = L_WO + 2400 * 16, L_Q = L_PART + 4 * 200 * 4,
              L_O = L_Q + 384, L_H = L_O + 768, L_FLAG = L_H + 256, L_END = L_FLAG + 16;
constexpr u32 SPIN_CAP = 1u << 22;

__device__ __forceinline__ float bf2f(u32 v) { return __uint_as_float(v << 16); }
__device__ __forceinline__ u32 f2bf(float f) { return (u32)__builtin_bit_cast(u16, (__bf16)f); }
__device__ __forceinline__ float rbf(float f) { return bf2f(f2bf(f)); }
__device__ __forceinline__ float lo16(u32 w) { return __uint_as_float(w << 16); }
__device__ __forceinline__ float hi16(u32 w) { return __uint_as_float(w & 0xffff0000u); }
__device__ __forceinline__ u32 pack2(float a, float b) { bf16x2 v = {(__bf16)a, (__bf16)b}; return __builtin_bit_cast(u32, v); }
__device__ __forceinline__ float dot2(u32 a, u32 b, float c) {
  return __builtin_amdgcn_fdot2_f32_bf16(__builtin_bit_cast(bf16x2, a), __builtin_bit_cast(bf16x2, b), c, false);
}
template <int CTRL> __device__ __forceinline__ float dpp(float x) {
  return __builtin_bit_cast(float, __builtin_amdgcn_mov_dpp(__builtin_bit_cast(int, x), CTRL, 0xf, 0xf, true));
}
constexpr int XOR1 = 0xB1, XOR2 = 0x4E, HMIR = 0x141, RMIR = 0x140, ROR8 = 0x128;
__device__ __forceinline__ float p16sum(float a, float b) {
  auto s = __builtin_amdgcn_permlane16_swap(__float_as_uint(a), __float_as_uint(b), false, false);
  return __uint_as_float(s[0]) + __uint_as_float(s[1]);
}
__device__ __forceinline__ float p32sum(float a, float b) {
  auto s = __builtin_amdgcn_permlane32_swap(__float_as_uint(a), __float_as_uint(b), false, false);
  return __uint_as_float(s[0]) + __uint_as_float(s[1]);
}
__device__ __forceinline__ float half_sum(float v) {
  v += dpp<XOR1>(v); v += dpp<XOR2>(v); v += dpp<HMIR>(v); v += dpp<RMIR>(v);
  return p16sum(v, v);
}
__device__ __forceinline__ float rl(float v, int lane) { return __uint_as_float(__builtin_amdgcn_readlane(__float_as_uint(v), lane)); }
__device__ __forceinline__ float silu(float z) { return z * __builtin_amdgcn_rcpf(1.f + __expf(-z)); }

__device__ __forceinline__ void put(GAS u64* p, float v, u32 thi, u32 tlo) {
  const u32 lo = ((__float_as_uint(v) + 0x80u) >> 8) | tlo;
  __hip_atomic_store(p, ((u64)thi << 32) | lo, RLX_AGENT);
}
__device__ __forceinline__ bool get(GAS u64* p, float& v, u32 thi, u32 tlo) {
  const u64 x = __hip_atomic_load(p, RLX_AGENT);
  v = __uint_as_float((u32)x << 8);
  return (u32)(x >> 32) == thi && ((u32)x & 0xff000000u) == tlo;
}

#define LD(dst, rs, voff, soff, IMM) asm volatile("buffer_load_dwordx4 %0, %1, %2, %3 offen offset:" #IMM " nt" : "=v"(dst) : "v"(voff), "s"(rs), "s"(soff))
#define LD3(arr, rs, soff) do { LD(arr[0], rs, loff, soff, 0); LD(arr[1], rs, loff, soff, 128); LD(arr[2], rs, loff, soff, 256); } while (0)
#define WLD3(w, N, dst, rs, soff) asm volatile("s_waitcnt vmcnt(" #N ")\n\tbuffer_load_dwordx4 %3, %6, %7, %8 offen nt\n\tbuffer_load_dwordx4 %4, %6, %7, %8 offen offset:128 nt\n\tbuffer_load_dwordx4 %5, %6, %7, %8 offen offset:256 nt" \
    : "+v"(w[0]), "+v"(w[1]), "+v"(w[2]), "=&v"(dst[0]), "=&v"(dst[1]), "=&v"(dst[2]) : "v"(loff), "s"(rs), "s"(soff))
#define WAIT3(arr, N) asm volatile("s_waitcnt vmcnt(" #N ")" : "+v"(arr[0]), "+v"(arr[1]), "+v"(arr[2]))
#define STAMP(i)

__global__ void __launch_bounds__(512, 2)
fused_kernel(const u16* __restrict__ x, const u16* __restrict__ Kc, const u16* __restrict__ Vc, const u16* __restrict__ wq, const u16* __restrict__ wo,
             const u16* __restrict__ wr, const u16* __restrict__ wg, const u16* __restrict__ wu, const u16* __restrict__ wd, u16* __restrict__ out, char* ws) {
  __shared__ __attribute__((aligned(16))) char lds_[L_END];
  LAS char* lds = (LAS char*)lds_;
  const int blk = blockIdx.x, tid = threadIdx.x, lane = tid & 63, wave = __builtin_amdgcn_readfirstlane(tid >> 6);
  const int hp = 4 * (blk & 7) + (blk >> 6), j = (blk >> 3) & 7, pid = hp * 8 + j;
  const int g = lane >> 4, i16 = lane & 15;
  GAS u64* G0 = (GAS u64*)(ws + WS_G0); GAS u64* G1 = (GAS u64*)(ws + WS_G1); GAS u64* G2 = (GAS u64*)(ws + WS_G2);
  STAMP(0);
  u32 thi = 0, tlo = 0; u64 epoch = 0;
  float fin_x = 0.f, fin_xn = 0.f, fin_rs = 0.f;

  if (wave >= 4) {
    const int sw = wave - 4, r = lane >> 3, c = lane & 7;
    i32x4 rk = {(int)(size_t)Kc, (int)((size_t)Kc >> 32), (int)KVB, 0x00020000};
    i32x4 rv = {(int)(size_t)Vc, (int)((size_t)Vc >> 32), (int)KVB, 0x00020000};
    u32 so = (u32)(((j >> 2) * H + hp) * SK + (j & 3) * 2048 + sw * 512) * 384u;
    const u32 loff = r * 384 + c * 16;
    u32x4 k0[3], v0[3], k1[3], v1[3];
    LD3(k0, rk, so); LD3(v0, rv, so);
    epoch = __hip_atomic_load((GAS u64*)ws, RLX_AGENT);
    so += 3072;
    WLD3(k0, 3, k1, rk, so);
    WLD3(v0, 3, v1, rv, so);
    if (wave == 4 && lane == 0) *(LAS u32*)(lds + L_FLAG) = 0;
    asm volatile("s_waitcnt lgkmcnt(0)\n\ts_barrier" ::: "memory");
    STAMP(1);
    u32x4 qf[3];
#pragma unroll
    for (int t = 0; t < 3; t++) qf[t] = *(LAS u32x4*)(lds + L_Q + 128 * t + 16 * c);
    float m = -INFINITY, l = 0.f;
    f32x2 acc[12];
#pragma unroll
    for (int t = 0; t < 12; t++) acc[t] = f32x2{0.f, 0.f};
    float p;
#define QK(kk) { \
      float d0 = 0.f, d1 = 0.f, d2 = 0.f; \
      _Pragma("unroll") for (int e = 0; e < 4; e++) { d0 = dot2(kk[0][e], qf[0][e], d0); d1 = dot2(kk[1][e], qf[1][e], d1); d2 = dot2(kk[2][e], qf[2][e], d2); } \
      float d = d0 + d1 + d2; \
      d += dpp<XOR1>(d); d += dpp<XOR2>(d); d += dpp<HMIR>(d); \
      const float t_ = rbf(rbf(d) * 0.07216878364870322f) * 1.4426950408889634f; \
      if (__builtin_amdgcn_ballot_w64(t_ > m) != 0) { \
        const float mn = fmaxf(m, t_), al = __builtin_amdgcn_exp2f(m - mn); \
        m = mn; l *= al; \
        _Pragma("unroll") for (int e = 0; e < 12; e++) acc[e] *= al; \
      } \
      p = __builtin_amdgcn_exp2f(t_ - m); l += p; \
      asm volatile("" : "+v"(p)); }
#define PV(vv) { \
      _Pragma("unroll") for (int t = 0; t < 3; t++) _Pragma("unroll") for (int e = 0; e < 4; e++) { \
        const u32 w_ = vv[t][e]; acc[t * 4 + e] += p * f32x2{lo16(w_), hi16(w_)}; } \
      asm volatile("" : "+v"(acc[0]), "+v"(acc[1]), "+v"(acc[2]), "+v"(acc[3]), "+v"(acc[4]), "+v"(acc[5]), "+v"(acc[6]), "+v"(acc[7]), "+v"(acc[8]), "+v"(acc[9]), "+v"(acc[10]), "+v"(acc[11])); }
    QK(k0) PV(v0)
#pragma unroll 1
    for (int it = 0; it < 31; it++) {
      so += 3072;
      WLD3(k1, 3, k0, rk, so); QK(k1)
      WLD3(v1, 3, v0, rv, so); PV(v1)
      so += 3072;
      WLD3(k0, 3, k1, rk, so); QK(k0)
      WLD3(v0, 3, v1, rv, so); PV(v0)
      if (WO_LATE && wave == 4 && it == 31 - (WO_LATE + 1) / 2) { if (lane == 0) *(volatile LAS u32*)(lds + L_FLAG) = 1; }
    }
    WAIT3(k1, 3); QK(k1)
    WAIT3(v1, 0); PV(v1)
    STAMP(2);
    float M = fmaxf(m, dpp<ROR8>(m));
    { auto s = __builtin_amdgcn_permlane16_swap(__float_as_uint(M), __float_as_uint(M), false, false); M = fmaxf(__uint_as_float(s[0]), __uint_as_float(s[1]));
      auto t = __builtin_amdgcn_permlane32_swap(__float_as_uint(M), __float_as_uint(M), false, false); M = fmaxf(__uint_as_float(t[0]), __uint_as_float(t[1])); }
    const float f = __builtin_amdgcn_exp2f(m - M);
    l *= f; l += dpp<ROR8>(l); l = p16sum(l, l); l = p32sum(l, l);
    float a24[24];
#pragma unroll
    for (int e = 0; e < 12; e++) { a24[2 * e] = acc[e][0] * f; a24[2 * e + 1] = acc[e][1] * f; }
    float a12[12], a6[6];
#pragma unroll
    for (int e = 0; e < 12; e++) a12[e] = p32sum(a24[e], a24[e + 12]);
#pragma unroll
    for (int e = 0; e < 6; e++) a6[e] = p16sum(a12[e], a12[e + 6]);
    const bool b3 = lane & 8;
    LAS float* part = (LAS float*)(lds + L_PART) + sw * 200;
    const int ai0 = 3 * ((lane >> 3) & 1) + 6 * ((lane >> 4) & 1) + 12 * (lane >> 5);
#pragma unroll
    for (int e = 0; e < 3; e++) {
      const float keep = b3 ? a6[e + 3] : a6[e], send = b3 ? a6[e] : a6[e + 3];
      const int ai = ai0 + e;
      part[64 * (ai >> 3) + 8 * c + (ai & 7)] = keep + dpp<ROR8>(send);
    }
    if (lane == 0) { part[192] = M; part[193] = l; }
  } else {
    epoch = __hip_atomic_load((GAS u64*)ws, RLX_AGENT);
    const int l32 = lane & 31, hb = lane >> 5;
    u32x2 xs[5], xa[5];
#pragma unroll
    for (int t = 0; t < 5; t++) xs[t] = *(const GAS u32x2*)((const GAS char*)x + hb * 1280 + 8 * (l32 + 32 * t));
#pragma unroll
    for (int t = 0; t < 5; t++) xa[t] = *(const GAS u32x2*)((const GAS char*)x + (lane & 1) * 1280 + (80 * j + 16 * t + 4 * g) * 2);
    u32x4 wv[15];
    if (wave < 3) {
      const GAS char* src = (const GAS char*)wq + (size_t)(80 * j + (lane >> 3)) * (H * DH * 2) + (hp * DH + 64 * wave) * 2 + (lane & 7) * 16;
#pragma unroll
      for (int t = 0; t < 10; t++) wv[t] = __builtin_nontemporal_load((const GAS u32x4*)(src + (size_t)t * 8 * (H * DH * 2)));
    } else {
#pragma unroll
      for (int t = 0; t < 15; t++) {
        const int id = t * 64 + lane, mat = id >= 480, rem = id - 480 * mat, row = rem / 6, ch = rem - 6 * row;
        const GAS char* src = (const GAS char*)(mat ? wu : wg) + (size_t)(80 * j + row) * (F * 2) + hp * 96 + ch * 16;
        wv[t] = __builtin_nontemporal_load((const GAS u32x4*)src);
      }
    }
    float xv[20], s = 0.f;
#pragma unroll
    for (int t = 0; t < 5; t++) { xv[4 * t] = lo16(xs[t][0]); xv[4 * t + 1] = hi16(xs[t][0]); xv[4 * t + 2] = lo16(xs[t][1]); xv[4 * t + 3] = hi16(xs[t][1]); }
#pragma unroll
    for (int t = 0; t < 20; t++) s += xv[t];
    const float mean = half_sum(s) * (1.f / D);
    float ss = 0.f;
#pragma unroll
    for (int t = 0; t < 20; t++) { const float dlt = xv[t] - mean; ss += dlt * dlt; }
    const float var = rbf(half_sum(ss) * (1.f / D)), mb = rbf(mean);
    const float rd = __builtin_amdgcn_rcpf(rbf(__builtin_amdgcn_sqrtf(rbf(var + 1e-5f))));
    const float mb0 = rl(mb, 0), mb1 = rl(mb, 32), rd0 = rl(rd, 0), rd1 = rl(rd, 32);
    const float mba = (lane & 1) ? mb1 : mb0, rda = (lane & 1) ? rd1 : rd0;
    s16x4 af[5];
#pragma unroll
    for (int t = 0; t < 5; t++) {
      u32x2 o; o[0] = pack2(rbf(lo16(xa[t][0]) - mba) * rda, rbf(hi16(xa[t][0]) - mba) * rda); o[1] = pack2(rbf(lo16(xa[t][1]) - mba) * rda, rbf(hi16(xa[t][1]) - mba) * rda);
      af[t] = __builtin_bit_cast(s16x4, o);
    }
    { const u64 tg = (epoch * 0x9E3779B97F4A7C15ull + 0x632BE59BD9B4E019ull) & 0xFFFFFFFFFFull; thi = (u32)(tg >> 8); tlo = (u32)tg << 24; }
    STAMP(1);
    const int q4 = i16 >> 2, p4 = lane & 3;
    if (wave < 3) {
      LAS char* wl = lds + L_WQ + wave * (80 * 160);
#pragma unroll
      for (int t = 0; t < 10; t++) *(LAS u32x4*)(wl + (8 * t + (lane >> 3)) * 160 + (lane & 7) * 16) = wv[t];
      f32x4 ac[4];
#pragma unroll
      for (int t = 0; t < 4; t++) {
        ac[t] = f32x4{0.f, 0.f, 0.f, 0.f};
#pragma unroll
        for (int ks = 0; ks < 5; ks++) {
          const s16x4 bf = __builtin_amdgcn_ds_read_tr16_b64_v4i16((LAS s16x4*)(wl + (16 * ks + 4 * g + q4) * 160 + (16 * t + 4 * p4) * 2));
          ac[t] = __builtin_amdgcn_mfma_f32_16x16x16bf16_1k(af[ks], bf, ac[t], 0, 0, 0);
        }
      }
      const float r0 = g == 0 ? ac[0][0] : g == 1 ? ac[1][0] : g == 2 ? ac[2][0] : ac[3][0];
      const float r1 = g == 0 ? ac[0][1] : g == 1 ? ac[1][1] : g == 2 ? ac[2][1] : ac[3][1];
      GAS u64* dst = G0 + (size_t)pid * 576 + 64 * wave + lane;
      put(dst, r0, thi, tlo); put(dst + 192, r1, thi, tlo);
      STAMP(2);
      GAS u64* src = G0 + (size_t)hp * 8 * 576 + (j >> 2) * 192 + 64 * wave + lane;
      float v[8];
      for (u32 spin = 0; spin < SPIN_CAP; spin++) {
        bool ok = true;
#pragma unroll
        for (int t = 0; t < 8; t++) ok &= get(src + t * 576, v[t], thi, tlo);
        if (__all(ok)) break;
      }
      const float qs = ((v[0] + v[1]) + (v[2] + v[3])) + ((v[4] + v[5]) + (v[6] + v[7]));
      *(LAS u16*)(lds + L_Q + (64 * wave + lane) * 2) = (u16)f2bf(qs);
      STAMP(3);
    } else {
      LAS char* wl = lds + L_GU;
#pragma unroll
      for (int t = 0; t < 15; t++) *(LAS u32x4*)(wl + (t * 64 + lane) * 16) = wv[t];
      f32x4 ac[6];
#pragma unroll
      for (int t = 0; t < 6; t++) {
        ac[t] = f32x4{0.f, 0.f, 0.f, 0.f};
#pragma unroll
        for (int ks = 0; ks < 5; ks++) {
          const s16x4 bf = __builtin_amdgcn_ds_read_tr16_b64_v4i16((LAS s16x4*)(wl + (t / 3) * 7680 + (16 * ks + 4 * g + q4) * 96 + (16 * (t % 3) + 4 * p4) * 2));
          ac[t] = __builtin_amdgcn_mfma_f32_16x16x16bf16_1k(af[ks], bf, ac[t], 0, 0, 0);
        }
      }
      if (g < 3) {
        GAS u64* dst = G0 + (size_t)pid * 576 + 384 + lane;
        put(dst, g == 0 ? ac[0][0] : g == 1 ? ac[1][0] : ac[2][0], thi, tlo);
        put(dst + 48, g == 0 ? ac[0][1] : g == 1 ? ac[1][1] : ac[2][1], thi, tlo);
        put(dst + 96, g == 0 ? ac[3][0] : g == 1 ? ac[4][0] : ac[5][0], thi, tlo);
        put(dst + 144, g == 0 ? ac[3][1] : g == 1 ? ac[4][1] : ac[5][1], thi, tlo);
      }
      STAMP(2);
    }
    asm volatile("s_waitcnt lgkmcnt(0)\n\ts_barrier" ::: "memory");
    if (wave == 3) {
      GAS u64* src = G0 + (size_t)hp * 8 * 576 + 384 + (lane < 48 ? lane : 0);
      float sm[4];
      for (u32 spin = 0; spin < SPIN_CAP; spin++) {
        bool ok = true;
#pragma unroll
        for (int e = 0; e < 4; e++) {
          float v[8];
#pragma unroll
          for (int t = 0; t < 8; t++) ok &= get(src + t * 576 + e * 48, v[t], thi, tlo);
          sm[e] = ((v[0] + v[1]) + (v[2] + v[3])) + ((v[4] + v[5]) + (v[6] + v[7]));
        }
        if (__all(ok)) break;
      }
      if (lane < 48) {
        *(LAS u16*)(lds + L_H + lane * 2) = (u16)f2bf(rbf(silu(rbf(sm[0]))) * rbf(sm[2]));
        *(LAS u16*)(lds + L_H + 128 + lane * 2) = (u16)f2bf(rbf(silu(rbf(sm[1]))) * rbf(sm[3]));
      }
    } else if (wave == 0) {
      float rs = 0.f;
#pragma unroll
      for (int t = 0; t < 5; t++) {
        const u32x2 w = *(const GAS u32x2*)((const GAS char*)wr + 8 * (l32 + 32 * t));
        rs += rbf(rbf(xv[4 * t] - mb) * rd) * lo16(w[0]) + rbf(rbf(xv[4 * t + 1] - mb) * rd) * hi16(w[0]) + rbf(rbf(xv[4 * t + 2] - mb) * rd) * lo16(w[1]) + rbf(rbf(xv[4 * t + 3] - mb) * rd) * hi16(w[1]);
      }
      rs = rbf(half_sum(rs));
      rs = rbf(__builtin_amdgcn_rcpf(1.f + __expf(-rs)));
      const int b = hp >> 4;
      fin_rs = b ? rl(rs, 32) : rl(rs, 0);
      fin_x = bf2f(x[b * D + 80 * j + 5 * (hp & 15) + (lane < 5 ? lane : 0)]);
      fin_xn = rbf(rbf(fin_x - (b ? mb1 : mb0)) * (b ? rd1 : rd0));
    }
    if (WO_LATE) { for (u32 spin = 0; spin < SPIN_CAP && *(volatile LAS u32*)(lds + L_FLAG) == 0; spin++) __builtin_amdgcn_s_sleep(4); }
    STAMP(4);
#pragma unroll
    for (int t = 0; t < 10; t++) {
      const int id = min(t * 256 + tid, 2399), row = id / 10, ch = id - 10 * row;
      const GAS char* src = (row < 192 ? (const GAS char*)wo + (size_t)(DH * hp + row) * (D * 2) : (const GAS char*)wd + (size_t)(48 * hp + row - 192) * (D * 2)) + 160 * j + 16 * ch;
      wv[t] = __builtin_nontemporal_load((const GAS u32x4*)src);
    }
#pragma unroll
    for (int t = 0; t < 10; t++) *(LAS u32x4*)(lds + L_WO + min(t * 256 + tid, 2399) * 16) = wv[t];
    STAMP(5);
  }
  asm volatile("s_waitcnt lgkmcnt(0)\n\ts_barrier" ::: "memory");
  STAMP(6);
  if (wave >= 4) { const u64 tg = (epoch * 0x9E3779B97F4A7C15ull + 0x632BE59BD9B4E019ull) & 0xFFFFFFFFFFull; thi = (u32)(tg >> 8); tlo = (u32)tg << 24; }
  s16x8 bw[6];
  const int tile = wave - 3;
  if (wave < 3) {
    const LAS float* part = (const LAS float*)(lds + L_PART);
    const int d = 64 * wave + lane;
    const float m0 = part[192], m1 = part[200 + 192], m2 = part[400 + 192], m3 = part[600 + 192];
    const float M = fmaxf(fmaxf(m0, m1), fmaxf(m2, m3));
    const float e0 = __builtin_amdgcn_exp2f(m0 - M), e1 = __builtin_amdgcn_exp2f(m1 - M), e2 = __builtin_amdgcn_exp2f(m2 - M), e3 = __builtin_amdgcn_exp2f(m3 - M);
    const float L = e0 * part[193] + e1 * part[200 + 193] + e2 * part[400 + 193] + e3 * part[600 + 193];
    const float o = (e0 * part[d] + e1 * part[200 + d] + e2 * part[400 + d] + e3 * part[600 + d]) * __builtin_amdgcn_rcpf(L);
    put(G1 + (size_t)pid * 256 + d, o, thi, tlo);
    if (tid == 0) put(G1 + (size_t)pid * 256 + 192, M + __builtin_amdgcn_logf(L), thi, tlo);
    STAMP(7);
    GAS u64* src = G1 + (size_t)hp * 8 * 256;
    float v[8], ls[8];
    for (u32 spin = 0; spin < SPIN_CAP; spin++) {
      bool ok = true;
#pragma unroll
      for (int t = 0; t < 8; t++) { ok &= get(src + t * 256 + d, v[t], thi, tlo); ok &= get(src + t * 256 + 192, ls[t], thi, tlo); }
      if (__all(ok)) break;
    }
#pragma unroll
    for (int b = 0; b < 2; b++) {
      const float mx = fmaxf(fmaxf(ls[4 * b], ls[4 * b + 1]), fmaxf(ls[4 * b + 2], ls[4 * b + 3]));
      const float w0 = __builtin_amdgcn_exp2f(ls[4 * b] - mx), w1 = __builtin_amdgcn_exp2f(ls[4 * b + 1] - mx), w2 = __builtin_amdgcn_exp2f(ls[4 * b + 2] - mx), w3 = __builtin_amdgcn_exp2f(ls[4 * b + 3] - mx);
      const float oo = (w0 * v[4 * b] + w1 * v[4 * b + 1] + w2 * v[4 * b + 2] + w3 * v[4 * b + 3]) * __builtin_amdgcn_rcpf((w0 + w1) + (w2 + w3));
      *(LAS u16*)(lds + L_O + b * 384 + d * 2) = (u16)f2bf(oo);
    }
    STAMP(8);
  } else {
    const int q4 = i16 >> 2, p4 = lane & 3;
    const LAS char* wl = lds + L_WO + (16 * tile + 4 * p4) * 2;
    f32x4 y = {0.f, 0.f, 0.f, 0.f};
#pragma unroll
    for (int ks = 0; ks < 3; ks++) {
      const s16x4 a = *(const LAS s16x4*)(lds + L_H + (lane & 1) * 128 + (16 * ks + 4 * g) * 2);
      const s16x4 bf = __builtin_amdgcn_ds_read_tr16_b64_v4i16((LAS s16x4*)(wl + (192 + 16 * ks + 4 * g + q4) * 160));
      y = __builtin_amdgcn_mfma_f32_16x16x16bf16_1k(a, bf, y, 0, 0, 0);
    }
    if (g < 2) put(G2 + (size_t)pid * 320 + 160 + g * 80 + 16 * tile + i16, g ? y[1] : y[0], thi, tlo);
#pragma unroll
    for (int ks = 0; ks < 6; ks++) {
      const s16x4 b0 = __builtin_amdgcn_ds_read_tr16_b64_v4i16((LAS s16x4*)(wl + (32 * ks + 8 * g + q4) * 160));
      const s16x4 b1 = __builtin_amdgcn_ds_read_tr16_b64_v4i16((LAS s16x4*)(wl + (32 * ks + 8 * g + 4 + q4) * 160));
      bw[ks] = __builtin_shufflevector(b0, b1, 0, 1, 2, 3, 4, 5, 6, 7);
    }
  }
  asm volatile("s_waitcnt lgkmcnt(0)\n\ts_barrier" ::: "memory");
  if (wave >= 3) {
    f32x4 s = {0.f, 0.f, 0.f, 0.f};
#pragma unroll
    for (int ks = 0; ks < 6; ks++) {
      const s16x8 a = *(const LAS s16x8*)(lds + L_O + (lane & 1) * 384 + 64 * ks + 16 * g);
      s = __builtin_amdgcn_mfma_f32_16x16x32_bf16(__builtin_bit_cast(bf16x8, a), __builtin_bit_cast(bf16x8, bw[ks]), s, 0, 0, 0);
    }
    if (g < 2) put(G2 + (size_t)pid * 320 + g * 80 + 16 * tile + i16, g ? s[1] : s[0], thi, tlo);
    STAMP(9);
  } else if (wave == 0) {
    GAS u64* src = G2 + (size_t)((lane & 31) * 8 + j) * 320 + (lane >> 5) * 160 + 5 * hp;
    float v[5];
    for (u32 spin = 0; spin < SPIN_CAP; spin++) {
      bool ok = true;
#pragma unroll
      for (int t = 0; t < 5; t++) ok &= get(src + t, v[t], thi, tlo);
      if (__all(ok)) break;
    }
    STAMP(9);
    float S = 0.f, Y = 0.f;
#pragma unroll
    for (int t = 0; t < 5; t++) { const float h = half_sum(v[t]); const float a = rl(h, 0), b = rl(h, 32); if (lane == t) { S = a; Y = b; } }
    const float z = rbf(fin_xn + rbf(rbf(Y) * fin_rs));
    const float res = fin_x + rbf(rbf(S) * rbf(silu(z)));
    if (lane < 5) out[(hp >> 4) * D + 80 * j + 5 * (hp & 15) + lane] = (u16)f2bf(res);
    if (blk == 0 && lane == 0) __hip_atomic_store((GAS u64*)ws, epoch + 1, RLX_AGENT);
    STAMP(10);
  }
}

extern "C" void kernel_launch(void* const* d_in, const int* in_sizes, int n_in, void* d_out, int out_size, void* d_ws, size_t ws_size, hipStream_t stream) {
  fused_kernel<<<256, 512, 0, stream>>>((const u16*)d_in[0], (const u16*)d_in[1], (const u16*)d_in[2], (const u16*)d_in[3], (const u16*)d_in[4],
                                        (const u16*)d_in[6], (const u16*)d_in[7], (const u16*)d_in[8], (const u16*)d_in[9], (u16*)d_out, (char*)d_ws);
}
```

```cpp
#define XW 3
#include <hip/hip_runtime.h>
#include <stdint.h>

typedef unsigned u32; typedef unsigned long long u64; typedef unsigned short u16;
typedef u32 u32x4 __attribute__((ext_vector_type(4)));
typedef u32 u32x2 __attribute__((ext_vector_type(2)));
typedef int i32x4 __attribute__((ext_vector_type(4)));
typedef float f32x2 __attribute__((ext_vector_type(2)));
typedef float f32x4 __attribute__((ext_vector_type(4)));
typedef short s16x4 __attribute__((ext_vector_type(4)));
typedef short s16x8 __attribute__((ext_vector_type(8)));
typedef __bf16 bf16x2 __attribute__((ext_vector_type(2)));
typedef __bf16 bf16x8 __attribute__((ext_vector_type(8)));
#define GAS __attribute__((address_space(1)))
#define LAS __attribute__((address_space(3)))
#define RLX_AGENT __ATOMIC_RELAXED, __HIP_MEMORY_SCOPE_AGENT

#define WO_LATE 0

#define BAR0 1
#define CHEAP_POLL 1
#define STR2(x) #x
#define STR(x) STR2(x)
constexpr int D = 640, H = 32, SK = 8192, DH = 192, F = 1536;
constexpr u32 KVB = 2u * 32u * 8192u * 192u * 2u;
constexpr size_t WS_G0 = 4096, WS_G1 = WS_G0 + 256 * 576 * 8, WS_G2 = WS_G1 + 256 * 256 * 8, WS_END = WS_G2 + 256 * 320 * 8;
constexpr int L_WQ = 0, L_GU = L_WQ + 3 * 80 * 160, L_WO = L_GU + 960 * 16, L_PART = L_WO + 2400 * 16, L_Q = L_PART + 4 * 200 * 4,
              L_O = L_Q + 384, L_H = L_O + 768, L_FLAG = L_H + 256, L_END = L_FLAG + 16;
constexpr u32 SPIN_CAP = 1u << 22;

__device__ __forceinline__ float bf2f(u32 v) { return __uint_as_float(v << 16); }
__device__ __forceinline__ u32 f2bf(float f) { return (u32)__builtin_bit_cast(u16, (__bf16)f); }
__device__ __forceinline__ float rbf(float f) { return bf2f(f2bf(f)); }
__device__ __forceinline__ float lo16(u32 w) { return __uint_as_float(w << 16); }
__device__ __forceinline__ float hi16(u32 w) { return __uint_as_float(w & 0xffff0000u); }
__device__ __forceinline__ u32 pack2(float a, float b) { bf16x2 v = {(__bf16)a, (__bf16)b}; return __builtin_bit_cast(u32, v); }
__device__ __forceinline__ float dot2(u32 a, u32 b, float c) {
  return __builtin_amdgcn_fdot2_f32_bf16(__builtin_bit_cast(bf16x2, a), __builtin_bit_cast(bf16x2, b), c, false);
}
template <int CTRL> __device__ __forceinline__ float dpp(float x) {
  return __builtin_bit_cast(float, __builtin_amdgcn_mov_dpp(__builtin_bit_cast(int, x), CTRL, 0xf, 0xf, true));
}
constexpr int XOR1 = 0xB1, XOR2 = 0x4E, HMIR = 0x141, RMIR = 0x140, ROR8 = 0x128;
__device__ __forceinline__ float p16sum(float a, float b) {
  auto s = __builtin_amdgcn_permlane16_swap(__float_as_uint(a), __float_as_uint(b), false, false);
  return __uint_as_float(s[0]) + __uint_as_float(s[1]);
}
__device__ __forceinline__ float p32sum(float a, float b) {
  auto s = __builtin_amdgcn_permlane32_swap(__float_as_uint(a), __float_as_uint(b), false, false);
  return __uint_as_float(s[0]) + __uint_as_float(s[1]);
}
__device__ __forceinline__ float half_sum(float v) {
  v += dpp<XOR1>(v); v += dpp<XOR2>(v); v += dpp<HMIR>(v); v += dpp<RMIR>(v);
  return p16sum(v, v);
}
__device__ __forceinline__ float rl(float v, int lane) { return __uint_as_float(__builtin_amdgcn_readlane(__float_as_uint(v), lane)); }
__device__ __forceinline__ float silu(float z) { return z * __builtin_amdgcn_rcpf(1.f + __expf(-z)); }

__device__ __forceinline__ void put(GAS u64* p, float v, u32 thi, u32 tlo) {
  const u32 lo = ((__float_as_uint(v) + 0x80u) >> 8) | tlo;
  __hip_atomic_store(p, ((u64)thi << 32) | lo, RLX_AGENT);
}
__device__ __forceinline__ bool get(GAS u64* p, float& v, u32 thi, u32 tlo) {
  const u64 x = __hip_atomic_load(p, RLX_AGENT);
  v = __uint_as_float((u32)x << 8);
  return (u32)(x >> 32) == thi && ((u32)x & 0xff000000u) == tlo;
}

#define LD(dst, rs, voff, soff, IMM) asm volatile("buffer_load_dwordx4 %0, %1, %2, %3 offen offset:" #IMM " nt" : "=v"(dst) : "v"(voff), "s"(rs), "s"(soff))
#define LD3(arr, rs, soff) do { LD(arr[0], rs, loff, soff, 0); LD(arr[1], rs, loff, soff, 128); LD(arr[2], rs, loff, soff, 256); } while (0)
#define WLD3(w, N, dst, rs, soff) asm volatile("s_waitcnt vmcnt(" #N ")\n\tbuffer_load_dwordx4 %3, %6, %7, %8 offen nt\n\tbuffer_load_dwordx4 %4, %6, %7, %8 offen offset:128 nt\n\tbuffer_load_dwordx4 %5, %6, %7, %8 offen offset:256 nt" \
    : "+v"(w[0]), "+v"(w[1]), "+v"(w[2]), "=&v"(dst[0]), "=&v"(dst[1]), "=&v"(dst[2]) : "v"(loff), "s"(rs), "s"(soff))
#define WAIT3(arr, N) asm volatile("s_waitcnt vmcnt(" #N ")" : "+v"(arr[0]), "+v"(arr[1]), "+v"(arr[2]))
#define STAMP(i)

__global__ void __launch_bounds__(512, 2)
fused_kernel(const u16* __restrict__ x, const u16* __restrict__ Kc, const u16* __restrict__ Vc, const u16* __restrict__ wq, const u16* __restrict__ wo,
             const u16* __restrict__ wr, const u16* __restrict__ wg, const u16* __restrict__ wu, const u16* __restrict__ wd, u16* __restrict__ out, char* ws) {
  __shared__ __attribute__((aligned(16))) char lds_[L_END];
  LAS char* lds = (LAS char*)lds_;
  const int blk = blockIdx.x, tid = threadIdx.x, lane = tid & 63, wave = __builtin_amdgcn_readfirstlane(tid >> 6);
  const int hp = 4 * (blk & 7) + (blk >> 6), j = (blk >> 3) & 7, pid = hp * 8 + j;
  const int g = lane >> 4, i16 = lane & 15;
  GAS u64* G0 = (GAS u64*)(ws + WS_G0); GAS u64* G1 = (GAS u64*)(ws + WS_G1); GAS u64* G2 = (GAS u64*)(ws + WS_G2);
  asm volatile("" :: "s"(x), "s"(Kc), "s"(Vc), "s"(wq), "s"(ws));
  STAMP(0);
  u32 thi = 0, tlo = 0; u64 epoch = 0;
  float fin_x = 0.f, fin_xn = 0.f, fin_rs = 0.f;

  if (wave >= 4) {
    const int sw = wave - 4, r = lane >> 3, c = lane & 7;
    i32x4 rk = {(int)(size_t)Kc, (int)((size_t)Kc >> 32), (int)KVB, 0x00020000};
    i32x4 rv = {(int)(size_t)Vc, (int)((size_t)Vc >> 32), (int)KVB, 0x00020000};
    u32 so = (u32)(((j >> 2) * H + hp) * SK + (j & 3) * 2048 + sw * 512) * 384u;
    const u32 loff = r * 384 + c * 16;
    u32x4 kr[4][3], vr[4][3];
    float p = 0.f;
    f32x2 acc[12];
#define LOADS3 "buffer_load_dwordx4 %0, %6, %7, %8 offen nt\n\tbuffer_load_dwordx4 %1, %6, %7, %8 offen offset:128 nt\n\tbuffer_load_dwordx4 %2, %6, %7, %8 offen offset:256 nt"
#define TLD0(dst, rs, soff) asm volatile("s_waitcnt vmcnt(" STR(XW) ")\n\tbuffer_load_dwordx4 %0, %3, %4, %5 offen nt\n\tbuffer_load_dwordx4 %1, %3, %4, %5 offen offset:128 nt\n\tbuffer_load_dwordx4 %2, %3, %4, %5 offen offset:256 nt" \
    : "=&v"(dst[0]), "=&v"(dst[1]), "=&v"(dst[2]) : "v"(loff), "s"(rs), "s"(soff))
#define TLDK(dst, nx, rs, soff) asm volatile("s_waitcnt vmcnt(" STR(XW) ")\n\t" LOADS3 \
    : "=&v"(dst[0]), "=&v"(dst[1]), "=&v"(dst[2]), "+v"(nx[0]), "+v"(nx[1]), "+v"(nx[2]) : "v"(loff), "s"(rs), "s"(soff), "v"(p))
#define TLDV(dst, nx, rs, soff) asm volatile("s_waitcnt vmcnt(" STR(XW) ")\n\t" LOADS3 \
    : "=&v"(dst[0]), "=&v"(dst[1]), "=&v"(dst[2]), "+v"(nx[0]), "+v"(nx[1]), "+v"(nx[2]) : "v"(loff), "s"(rs), "s"(soff), \
      "v"(acc[0]), "v"(acc[1]), "v"(acc[2]), "v"(acc[3]), "v"(acc[4]), "v"(acc[5]), "v"(acc[6]), "v"(acc[7]), "v"(acc[8]), "v"(acc[9]), "v"(acc[10]), "v"(acc[11]))
    if (BAR0) asm volatile("s_barrier" ::: "memory");
    TLD0(kr[0], rk, so); TLD0(vr[0], rv, so);
    TLD0(kr[1], rk, so + 3072); TLD0(vr[1], rv, so + 3072);
    TLD0(kr[2], rk, so + 6144); TLD0(vr[2], rv, so + 6144);
    TLD0(kr[3], rk, so + 9216); TLDK(vr[3], kr[0], rv, so + 9216);
    STAMP(3);
    if (wave == 4 && lane == 0) *(LAS u32*)(lds + L_FLAG) = 0;
    asm volatile("s_waitcnt lgkmcnt(0)\n\ts_barrier" ::: "memory");
    STAMP(1);
    u32x4 qf[3];
#pragma unroll
    for (int t = 0; t < 3; t++) qf[t] = *(LAS u32x4*)(lds + L_Q + 128 * t + 16 * c);
    float m = -INFINITY, l = 0.f;
#pragma unroll
    for (int t = 0; t < 12; t++) acc[t] = f32x2{0.f, 0.f};
#define QK(kk) { \
      float d0 = 0.f, d1 = 0.f, d2 = 0.f; \
      _Pragma("unroll") for (int e = 0; e < 4; e++) { d0 = dot2(kk[0][e], qf[0][e], d0); d1 = dot2(kk[1][e], qf[1][e], d1); d2 = dot2(kk[2][e], qf[2][e], d2); } \
      float d = d0 + d1 + d2; \
      d += dpp<XOR1>(d); d += dpp<XOR2>(d); d += dpp<HMIR>(d); \
      const float t_ = rbf(rbf(d) * 0.07216878364870322f) * 1.4426950408889634f; \
      if (__builtin_amdgcn_ballot_w64(t_ > m) != 0) { \
        const float mn = fmaxf(m, t_), al = __builtin_amdgcn_exp2f(m - mn); \
        m = mn; l *= al; \
        _Pragma("unroll") for (int e = 0; e < 12; e++) acc[e] *= al; \
      } \
      p = __builtin_amdgcn_exp2f(t_ - m); l += p; }
#define PV(vv) { \
      _Pragma("unroll") for (int t = 0; t < 3; t++) _Pragma("unroll") for (int e = 0; e < 4; e++) { \
        const u32 w_ = vv[t][e]; acc[t * 4 + e] += p * f32x2{lo16(w_), hi16(w_)}; } }
#pragma unroll 1
    for (int it = 0; it < 15; it++) {
      so += 4 * 3072;
#pragma unroll
      for (int s = 0; s < 4; s++) {
        QK(kr[s]) TLDK(kr[s], vr[s], rk, so + s * 3072);
        PV(vr[s]) TLDV(vr[s], kr[(s + 1) & 3], rv, so + s * 3072);
      }
      if (WO_LATE && wave == 4 && it == 15 - WO_LATE) { if (lane == 0) *(volatile LAS u32*)(lds + L_FLAG) = 1; }
    }
    QK(kr[0]) WAIT3(vr[0], 18); PV(vr[0])
    WAIT3(kr[1], 15); QK(kr[1]) WAIT3(vr[1], 12); PV(vr[1])
    WAIT3(kr[2], 9); QK(kr[2]) WAIT3(vr[2], 6); PV(vr[2])
    WAIT3(kr[3], 3); QK(kr[3]) WAIT3(vr[3], 0); PV(vr[3])
    STAMP(2);
    float M = fmaxf(m, dpp<ROR8>(m));
    { auto s = __builtin_amdgcn_permlane16_swap(__float_as_uint(M), __float_as_uint(M), false, false); M = fmaxf(__uint_as_float(s[0]), __uint_as_float(s[1]));
      auto t = __builtin_amdgcn_permlane32_swap(__float_as_uint(M), __float_as_uint(M), false, false); M = fmaxf(__uint_as_float(t[0]), __uint_as_float(t[1])); }
    const float f = __builtin_amdgcn_exp2f(m - M);
    l *= f; l += dpp<ROR8>(l); l = p16sum(l, l); l = p32sum(l, l);
    float a24[24];
#pragma unroll
    for (int e = 0; e < 12; e++) { a24[2 * e] = acc[e][0] * f; a24[2 * e + 1] = acc[e][1] * f; }
    float a12[12], a6[6];
#pragma unroll
    for (int e = 0; e < 12; e++) a12[e] = p32sum(a24[e], a24[e + 12]);
#pragma unroll
    for (int e = 0; e < 6; e++) a6[e] = p16sum(a12[e], a12[e + 6]);
    const bool b3 = lane & 8;
    LAS float* part = (LAS float*)(lds + L_PART) + sw * 200;
    const int ai0 = 3 * ((lane >> 3) & 1) + 6 * ((lane >> 4) & 1) + 12 * (lane >> 5);
#pragma unroll
    for (int e = 0; e < 3; e++) {
      const float keep = b3 ? a6[e + 3] : a6[e], send = b3 ? a6[e] : a6[e + 3];
      const int ai = ai0 + e;
      part[64 * (ai >> 3) + 8 * c + (ai & 7)] = keep + dpp<ROR8>(send);
    }
    if (lane == 0) { part[192] = M; part[193] = l; }
  } else {
    const int l32 = lane & 31, hb = lane >> 5;
    u32x2 xs[5], xa[5], ep;
    u32x4 wv[15];
    {
      const i32x4 rx = {(int)(size_t)x, (int)((size_t)x >> 32), 2 * D * 2, 0x00020000};
      const u32 o1 = hb * 1280 + 8 * l32, o2 = (lane & 1) * 1280 + 8 * g, zero = 0;
      asm volatile("buffer_load_dwordx2 %0, %11, %13, 0 offen\n\tbuffer_load_dwordx2 %1, %11, %13, 0 offen offset:256\n\tbuffer_load_dwordx2 %2, %11, %13, 0 offen offset:512\n\t"
                   "buffer_load_dwordx2 %3, %11, %13, 0 offen offset:768\n\tbuffer_load_dwordx2 %4, %11, %13, 0 offen offset:1024\n\t"
                   "buffer_load_dwordx2 %5, %12, %13, %14 offen\n\tbuffer_load_dwordx2 %6, %12, %13, %14 offen offset:32\n\tbuffer_load_dwordx2 %7, %12, %13, %14 offen offset:64\n\t"
                   "buffer_load_dwordx2 %8, %12, %13, %14 offen offset:96\n\tbuffer_load_dwordx2 %9, %12, %13, %14 offen offset:128\n\t"
                   "global_load_dwordx2 %10, %15, %16 sc1"
                   : "=&v"(xs[0]), "=&v"(xs[1]), "=&v"(xs[2]), "=&v"(xs[3]), "=&v"(xs[4]), "=&v"(xa[0]), "=&v"(xa[1]), "=&v"(xa[2]), "=&v"(xa[3]), "=&v"(xa[4]), "=&v"(ep)
                   : "v"(o1), "v"(o2), "s"(rx), "s"(160 * j), "v"(zero), "s"(ws) : "memory");
      const i32x4 rq = {(int)(size_t)wq, (int)((size_t)wq >> 32), wave < 3 ? D * H * DH * 2 : 0, 0x00020000};
      const u32 vo = (lane >> 3) * (H * DH * 2) + (lane & 7) * 16, s0 = 80 * j * (H * DH * 2) + (hp * DH + 64 * wave) * 2;
#pragma unroll
      for (int t = 0; t < 10; t++) LD(wv[t], rq, vo, s0 + t * 8 * (H * DH * 2), 0);
    }
    if (BAR0) asm volatile("s_barrier" ::: "memory");
    asm volatile("s_waitcnt vmcnt(10)" : "+v"(xs[0]), "+v"(xs[1]), "+v"(xs[2]), "+v"(xs[3]), "+v"(xs[4]), "+v"(xa[0]), "+v"(xa[1]), "+v"(xa[2]), "+v"(xa[3]), "+v"(xa[4]), "+v"(ep));
    epoch = ((u64)ep[1] << 32) | ep[0];
    float xv[20], s = 0.f;
#pragma unroll
    for (int t = 0; t < 5; t++) { xv[4 * t] = lo16(xs[t][0]); xv[4 * t + 1] = hi16(xs[t][0]); xv[4 * t + 2] = lo16(xs[t][1]); xv[4 * t + 3] = hi16(xs[t][1]); }
#pragma unroll
    for (int t = 0; t < 20; t++) s += xv[t];
    const float mean = half_sum(s) * (1.f / D);
    float ss = 0.f;
#pragma unroll
    for (int t = 0; t < 20; t++) { const float dlt = xv[t] - mean; ss += dlt * dlt; }
    const float var = rbf(half_sum(ss) * (1.f / D)), mb = rbf(mean);
    const float rd = __builtin_amdgcn_rcpf(rbf(__builtin_amdgcn_sqrtf(rbf(var + 1e-5f))));
    const float mb0 = rl(mb, 0), mb1 = rl(mb, 32), rd0 = rl(rd, 0), rd1 = rl(rd, 32);
    const float mba = (lane & 1) ? mb1 : mb0, rda = (lane & 1) ? rd1 : rd0;
    s16x4 af[5];
#pragma unroll
    for (int t = 0; t < 5; t++) {
      u32x2 o; o[0] = pack2(rbf(lo16(xa[t][0]) - mba) * rda, rbf(hi16(xa[t][0]) - mba) * rda); o[1] = pack2(rbf(lo16(xa[t][1]) - mba) * rda, rbf(hi16(xa[t][1]) - mba) * rda);
      af[t] = __builtin_bit_cast(s16x4, o);
    }
    { const u64 tg = (epoch * 0x9E3779B97F4A7C15ull + 0x632BE59BD9B4E019ull) & 0xFFFFFFFFFFull; thi = (u32)(tg >> 8); tlo = (u32)tg << 24; }
    STAMP(1);
    const int q4 = i16 >> 2, p4 = lane & 3;
    if (wave < 3) {
      LAS char* wl = lds + L_WQ + wave * (80 * 160);
      asm volatile("s_waitcnt vmcnt(0)" : "+v"(wv[0]), "+v"(wv[1]), "+v"(wv[2]), "+v"(wv[3]), "+v"(wv[4]), "+v"(wv[5]), "+v"(wv[6]), "+v"(wv[7]), "+v"(wv[8]), "+v"(wv[9]), "+v"(af[0]), "+v"(af[1]), "+v"(af[2]), "+v"(af[3]), "+v"(af[4]));
      STAMP(11);
#pragma unroll
      for (int t = 0; t < 10; t++) *(LAS u32x4*)(wl + (8 * t + (lane >> 3)) * 160 + (lane & 7) * 16) = wv[t];
      f32x4 ac[4];
#pragma unroll
      for (int t = 0; t < 4; t++) {
        ac[t] = f32x4{0.f, 0.f, 0.f, 0.f};
#pragma unroll
        for (int ks = 0; ks < 5; ks++) {
          const s16x4 bf = __builtin_amdgcn_ds_read_tr16_b64_v4i16((LAS s16x4*)(wl + (16 * ks + 4 * g + q4) * 160 + (16 * t + 4 * p4) * 2));
          ac[t] = __builtin_amdgcn_mfma_f32_16x16x16bf16_1k(af[ks], bf, ac[t], 0, 0, 0);
        }
      }
      const float r0 = g == 0 ? ac[0][0] : g == 1 ? ac[1][0] : g == 2 ? ac[2][0] : ac[3][0];
      const float r1 = g == 0 ? ac[0][1] : g == 1 ? ac[1][1] : g == 2 ? ac[2][1] : ac[3][1];
      GAS u64* dst = G0 + (size_t)pid * 576 + 64 * wave + lane;
      put(dst, r0, thi, tlo); put(dst + 192, r1, thi, tlo);
      STAMP(2);
      GAS u64* src = G0 + (size_t)hp * 8 * 576 + (j >> 2) * 192 + 64 * wave + lane;
      float v[8];
      if (CHEAP_POLL) {
        GAS u64* pw = G0 + (size_t)(hp * 8 + (lane & 7)) * 576 + (j >> 2) * 192 + 64 * wave + 63;
        for (u32 spin = 0; spin < SPIN_CAP; spin++) { if (__all(get(pw, v[0], thi, tlo))) break; }
      }
      for (u32 spin = 0; spin < SPIN_CAP; spin++) {
        bool ok = true;
#pragma unroll
        for (int t = 0; t < 8; t++) ok &= get(src + t * 576, v[t], thi, tlo);
        if (__all(ok)) break;
      }
      const float qs = ((v[0] + v[1]) + (v[2] + v[3])) + ((v[4] + v[5]) + (v[6] + v[7]));
      *(LAS u16*)(lds + L_Q + (64 * wave + lane) * 2) = (u16)f2bf(qs);
      STAMP(3);
    } else {
      asm volatile("s_waitcnt vmcnt(0)" :: "v"(wv[0]), "v"(wv[1]), "v"(wv[2]), "v"(wv[3]), "v"(wv[4]), "v"(wv[5]), "v"(wv[6]), "v"(wv[7]), "v"(wv[8]), "v"(wv[9]));
      if (lane == 0) { *(LAS u32*)(lds + L_FLAG + 4) = thi; *(LAS u32*)(lds + L_FLAG + 8) = tlo; }
    }
    asm volatile("s_waitcnt lgkmcnt(0)\n\ts_barrier" ::: "memory");
    if (wave == 3) {
#pragma unroll
      for (int t = 0; t < 15; t++) {
        const int id = t * 64 + lane, mat = id >= 480, rem = id - 480 * mat, row = rem / 6, ch = rem - 6 * row;
        const GAS char* src = (const GAS char*)(mat ? wu : wg) + (size_t)(80 * j + row) * (F * 2) + hp * 96 + ch * 16;
        wv[t] = __builtin_nontemporal_load((const GAS u32x4*)src);
      }
      LAS char* wl = lds + L_GU;
#pragma unroll
      for (int t = 0; t < 15; t++) *(LAS u32x4*)(wl + (t * 64 + lane) * 16) = wv[t];
      f32x4 ac[6];
#pragma unroll
      for (int t = 0; t < 6; t++) {
        ac[t] = f32x4{0.f, 0.f, 0.f, 0.f};
#pragma unroll
        for (int ks = 0; ks < 5; ks++) {
          const s16x4 bf = __builtin_amdgcn_ds_read_tr16_b64_v4i16((LAS s16x4*)(wl + (t / 3) * 7680 + (16 * ks + 4 * g + q4) * 96 + (16 * (t % 3) + 4 * p4) * 2));
          ac[t] = __builtin_amdgcn_mfma_f32_16x16x16bf16_1k(af[ks], bf, ac[t], 0, 0, 0);
        }
      }
      if (g < 3) {
        GAS u64* dst = G0 + (size_t)pid * 576 + 384 + lane;
        put(dst, g == 0 ? ac[0][0] : g == 1 ? ac[1][0] : ac[2][0], thi, tlo);
        put(dst + 48, g == 0 ? ac[0][1] : g == 1 ? ac[1][1] : ac[2][1], thi, tlo);
        put(dst + 96, g == 0 ? ac[3][0] : g == 1 ? ac[4][0] : ac[5][0], thi, tlo);
        put(dst + 144, g == 0 ? ac[3][1] : g == 1 ? ac[4][1] : ac[5][1], thi, tlo);
      }
      STAMP(2);
      __builtin_amdgcn_s_sleep(127); __builtin_amdgcn_s_sleep(127);
      GAS u64* src = G0 + (size_t)hp * 8 * 576 + 384 + (lane < 48 ? lane : 0);
      float sm[4];
      for (u32 spin = 0; spin < SPIN_CAP; spin++) {
        bool ok = true;
#pragma unroll
        for (int e = 0; e < 4; e++) {
          float v[8];
#pragma unroll
          for (int t = 0; t < 8; t++) ok &= get(src + t * 576 + e * 48, v[t], thi, tlo);
          sm[e] = ((v[0] + v[1]) + (v[2] + v[3])) + ((v[4] + v[5]) + (v[6] + v[7]));
        }
        if (__all(ok)) break;
        __builtin_amdgcn_s_sleep(64);
      }
      if (lane < 48) {
        *(LAS u16*)(lds + L_H + lane * 2) = (u16)f2bf(rbf(silu(rbf(sm[0]))) * rbf(sm[2]));
        *(LAS u16*)(lds + L_H + 128 + lane * 2) = (u16)f2bf(rbf(silu(rbf(sm[1]))) * rbf(sm[3]));
      }
    } else if (wave == 0) {
      float rs = 0.f;
#pragma unroll
      for (int t = 0; t < 5; t++) {
        const u32x2 w = *(const GAS u32x2*)((const GAS char*)wr + 8 * (l32 + 32 * t));
        rs += rbf(rbf(xv[4 * t] - mb) * rd) * lo16(w[0]) + rbf(rbf(xv[4 * t + 1] - mb) * rd) * hi16(w[0]) + rbf(rbf(xv[4 * t + 2] - mb) * rd) * lo16(w[1]) + rbf(rbf(xv[4 * t + 3] - mb) * rd) * hi16(w[1]);
      }
      rs = rbf(half_sum(rs));
      rs = rbf(__builtin_amdgcn_rcpf(1.f + __expf(-rs)));
      const int b = hp >> 4;
      fin_rs = b ? rl(rs, 32) : rl(rs, 0);
      fin_x = bf2f(x[b * D + 80 * j + 5 * (hp & 15) + (lane < 5 ? lane : 0)]);
      fin_xn = rbf(rbf(fin_x - (b ? mb1 : mb0)) * (b ? rd1 : rd0));
    }
    if (WO_LATE) { for (u32 spin = 0; spin < SPIN_CAP && *(volatile LAS u32*)(lds + L_FLAG) == 0; spin++) __builtin_amdgcn_s_sleep(4); }
    STAMP(4);
#pragma unroll
    for (int t = 0; t < 10; t++) {
      const int id = min(t * 256 + tid, 2399), row = id / 10, ch = id - 10 * row;
      const GAS char* src = (row < 192 ? (const GAS char*)wo + (size_t)(DH * hp + row) * (D * 2) : (const GAS char*)wd + (size_t)(48 * hp + row - 192) * (D * 2)) + 160 * j + 16 * ch;
      wv[t] = __builtin_nontemporal_load((const GAS u32x4*)src);
    }
#pragma unroll
    for (int t = 0; t < 10; t++) *(LAS u32x4*)(lds + L_WO + min(t * 256 + tid, 2399) * 16) = wv[t];
    STAMP(5);
  }
  asm volatile("s_waitcnt lgkmcnt(0)\n\ts_barrier" ::: "memory");
  STAMP(6);
  if (wave >= 4) { thi = *(LAS u32*)(lds + L_FLAG + 4); tlo = *(LAS u32*)(lds + L_FLAG + 8); }
  s16x8 bw[6];
  const int tile = wave - 3;
  if (wave < 3) {
    const LAS float* part = (const LAS float*)(lds + L_PART);
    const int d = 64 * wave + lane;
    const float m0 = part[192], m1 = part[200 + 192], m2 = part[400 + 192], m3 = part[600 + 192];
    const float M = fmaxf(fmaxf(m0, m1), fmaxf(m2, m3));
    const float e0 = __builtin_amdgcn_exp2f(m0 - M), e1 = __builtin_amdgcn_exp2f(m1 - M), e2 = __builtin_amdgcn_exp2f(m2 - M), e3 = __builtin_amdgcn_exp2f(m3 - M);
    const float L = e0 * part[193] + e1 * part[200 + 193] + e2 * part[400 + 193] + e3 * part[600 + 193];
    const float o = (e0 * part[d] + e1 * part[200 + d] + e2 * part[400 + d] + e3 * part[600 + d]) * __builtin_amdgcn_rcpf(L);
    put(G1 + (size_t)pid * 256 + d, o, thi, tlo);
    if (tid == 0) put(G1 + (size_t)pid * 256 + 192, M + __builtin_amdgcn_logf(L), thi, tlo);
    STAMP(7);
    GAS u64* src = G1 + (size_t)hp * 8 * 256;
    float v[8], ls[8];
    for (u32 spin = 0; spin < SPIN_CAP; spin++) {
      bool ok = true;
#pragma unroll
      for (int t = 0; t < 8; t++) { ok &= get(src + t * 256 + d, v[t], thi, tlo); ok &= get(src + t * 256 + 192, ls[t], thi, tlo); }
      if (__all(ok)) break;
    }
#pragma unroll
    for (int b = 0; b < 2; b++) {
      const float mx = fmaxf(fmaxf(ls[4 * b], ls[4 * b + 1]), fmaxf(ls[4 * b + 2], ls[4 * b + 3]));
      const float w0 = __builtin_amdgcn_exp2f(ls[4 * b] - mx), w1 = __builtin_amdgcn_exp2f(ls[4 * b + 1] - mx), w2 = __builtin_amdgcn_exp2f(ls[4 * b + 2] - mx), w3 = __builtin_amdgcn_exp2f(ls[4 * b + 3] - mx);
      const float oo = (w0 * v[4 * b] + w1 * v[4 * b + 1] + w2 * v[4 * b + 2] + w3 * v[4 * b + 3]) * __builtin_amdgcn_rcpf((w0 + w1) + (w2 + w3));
      *(LAS u16*)(lds + L_O + b * 384 + d * 2) = (u16)f2bf(oo);
    }
    STAMP(8);
  } else {
    const int q4 = i16 >> 2, p4 = lane & 3;
    const LAS char* wl = lds + L_WO + (16 * tile + 4 * p4) * 2;
    f32x4 y = {0.f, 0.f, 0.f, 0.f};
#pragma unroll
    for (int ks = 0; ks < 3; ks++) {
      const s16x4 a = *(const LAS s16x4*)(lds + L_H + (lane & 1) * 128 + (16 * ks + 4 * g) * 2);
      const s16x4 bf = __builtin_amdgcn_ds_read_tr16_b64_v4i16((LAS s16x4*)(wl + (192 + 16 * ks + 4 * g + q4) * 160));
      y = __builtin_amdgcn_mfma_f32_16x16x16bf16_1k(a, bf, y, 0, 0, 0);
    }
    if (g < 2) put(G2 + (size_t)pid * 320 + 160 + g * 80 + 16 * tile + i16, g ? y[1] : y[0], thi, tlo);
#pragma unroll
    for (int ks = 0; ks < 6; ks++) {
      const s16x4 b0 = __builtin_amdgcn_ds_read_tr16_b64_v4i16((LAS s16x4*)(wl + (32 * ks + 8 * g + q4) * 160));
      const s16x4 b1 = __builtin_amdgcn_ds_read_tr16_b64_v4i16((LAS s16x4*)(wl + (32 * ks + 8 * g + 4 + q4) * 160));
      bw[ks] = __builtin_shufflevector(b0, b1, 0, 1, 2, 3, 4, 5, 6, 7);
    }
  }
  asm volatile("s_waitcnt lgkmcnt(0)\n\ts_barrier" ::: "memory");
  if (wave >= 3) {
    f32x4 s = {0.f, 0.f, 0.f, 0.f};
#pragma unroll
    for (int ks = 0; ks < 6; ks++) {
      const s16x8 a = *(const LAS s16x8*)(lds + L_O + (lane & 1) * 384 + 64 * ks + 16 * g);
      s = __builtin_amdgcn_mfma_f32_16x16x32_bf16(__builtin_bit_cast(bf16x8, a), __builtin_bit_cast(bf16x8, bw[ks]), s, 0, 0, 0);
    }
    if (g < 2) put(G2 + (size_t)pid * 320 + g * 80 + 16 * tile + i16, g ? s[1] : s[0], thi, tlo);
    STAMP(9);
  } else if (wave == 0) {
    GAS u64* src = G2 + (size_t)((lane & 31) * 8 + j) * 320 + (lane >> 5) * 160 + 5 * hp;
    float v[5];
    for (u32 spin = 0; spin < SPIN_CAP; spin++) {
      bool ok = true;
#pragma unroll
      for (int t = 0; t < 5; t++) ok &= get(src + t, v[t], thi, tlo);
      if (__all(ok)) break;
    }
    STAMP(9);
    float S = 0.f, Y = 0.f;
#pragma unroll
    for (int t = 0; t < 5; t++) { const float h = half_sum(v[t]); const float a = rl(h, 0), b = rl(h, 32); if (lane == t) { S = a; Y = b; } }
    const float z = rbf(fin_xn + rbf(rbf(Y) * fin_rs));
    const float res = fin_x + rbf(rbf(S) * rbf(silu(z)));
    if (lane < 5) out[(hp >> 4) * D + 80 * j + 5 * (hp & 15) + lane] = (u16)f2bf(res);
    if (blk == 0 && lane == 0) __hip_atomic_store((GAS u64*)ws, epoch + 1, RLX_AGENT);
    STAMP(10);
  }
}

__global__ void probe_kernel(const char* base, unsigned* sink) {
  unsigned off = 0, acc = 0;
  for (int i = 0; i < 160; i++) {
    unsigned v = *(const volatile unsigned*)(base + off);
    acc ^= v;
    unsigned z; asm volatile("v_and_b32 %0, 0, %1" : "=v"(z) : "v"(v));
    off += 8192 + 128 + z;
  }
  if (acc == 0x13572468u) sink[0] = acc;
}
extern "C" void kernel_launch(void* const* d_in, const int* in_sizes, int n_in, void* d_out, int out_size, void* d_ws, size_t ws_size, hipStream_t stream) {
  fused_kernel<<<256, 512, 0, stream>>>((const u16*)d_in[0], (const u16*)d_in[1], (const u16*)d_in[2], (const u16*)d_in[3], (const u16*)d_in[4],
                                        (const u16*)d_in[6], (const u16*)d_in[7], (const u16*)d_in[8], (const u16*)d_in[9], (u16*)d_out, (char*)d_ws);
}
```

```cpp
#include <hip/hip_runtime.h>
#include <stdint.h>

typedef unsigned u32; typedef unsigned long long u64; typedef unsigned short u16;
typedef u32 u32x4 __attribute__((ext_vector_type(4)));
typedef u32 u32x2 __attribute__((ext_vector_type(2)));
typedef int i32x4 __attribute__((ext_vector_type(4)));
typedef float f32x2 __attribute__((ext_vector_type(2)));
typedef float f32x4 __attribute__((ext_vector_type(4)));
typedef short s16x4 __attribute__((ext_vector_type(4)));
typedef short s16x8 __attribute__((ext_vector_type(8)));
typedef __bf16 bf16x2 __attribute__((ext_vector_type(2)));
typedef __bf16 bf16x8 __attribute__((ext_vector_type(8)));
typedef __amdgpu_buffer_rsrc_t rsrc_t;
#define GAS __attribute__((address_space(1)))
#define LAS __attribute__((address_space(3)))
#define RLX_AGENT __ATOMIC_RELAXED, __HIP_MEMORY_SCOPE_AGENT
#define RLX_WG __ATOMIC_RELAXED, __HIP_MEMORY_SCOPE_WORKGROUP

constexpr int D = 640, H = 32, SK = 8192, DH = 192, F = 1536;
constexpr u32 KVB = 2u * 32u * 8192u * 192u * 2u;

constexpr u32 TAU = 1320, TAUV = 1306, TAU_SLOW = 1432;
constexpr u32 G_OFF = 200;
constexpr int A_G = 4, A_T = 850, A_C = 340;
constexpr int T_LO = 950, T_HI = 1500;
constexpr u32 T_Q = 600, T_H = 530, T_Y = 1030, T_LSE = 300;
constexpr int G1_DELAY = 17, G2_DELAY = 12, PSLEEP = 1;
constexpr u32 R_T = 110, R_M = 80;
constexpr float R_SK = 10.f, R_SV = 11.f, R_SMAX = 70.f, R_LK = 42.f, R_LV = 11.f, R_STALL = 130.f;
constexpr float R_SHK = 1.05f, R_SHV = 1.4f, R_CENS = 90.f;
constexpr u32 T_LATE = 11;
constexpr u32 ADV = 9;
constexpr u32 CODE_BYTES = 24320;

constexpr u32 S0 = 576, S1 = 208, S2 = 320;
constexpr u32 IH = 64, I0 = 1024, I1 = I0 + 256 * S0, I2 = I1 + 256 * S1, IEND = I2 + 256 * S2;
constexpr int TQ = 4, TO = 8;
constexpr u32 SPIN_CAP = 1u << 17;
constexpr int L_WQ = 0, L_GU = L_WQ + 3 * 80 * 160, L_WO = L_GU + 960 * 16, L_PART = L_WO + 248 * 160, L_Q = L_PART + 8 * 192 * 4, L_O = L_Q + 384,
              L_H = L_O + 768, L_FIN = L_H + 256, L_ML = L_FIN + 128, L_FW = L_ML + 64, L_CTL = L_FW + 32, L_END = L_CTL + 80;
constexpr int C_ML = L_CTL, C_LATE = L_CTL + 4, C_WO = L_CTL + 8, C_TAGOK = L_CTL + 12, C_SAMP = L_CTL + 16, C_DEC = L_CTL + 24,
              C_FIRST = L_CTL + 32, C_TAG = L_CTL + 40, C_EPOCH = L_CTL + 48, C_ANCHOR = L_CTL + 56;

__device__ __forceinline__ float bf2f(u32 v) { return __uint_as_float(v << 16); }
__device__ __forceinline__ u32 f2bf(float f) { return (u32)__builtin_bit_cast(u16, (__bf16)f); }
__device__ __forceinline__ float rbf(float f) { return bf2f(f2bf(f)); }
__device__ __forceinline__ float lo16(u32 w) { return __uint_as_float(w << 16); }
__device__ __forceinline__ float hi16(u32 w) { return __uint_as_float(w & 0xffff0000u); }
__device__ __forceinline__ u32 pack2(float a, float b) { bf16x2 v = {(__bf16)a, (__bf16)b}; return __builtin_bit_cast(u32, v); }
__device__ __forceinline__ float dot2(u32 a, u32 b, float c) {
  return __builtin_amdgcn_fdot2_f32_bf16(__builtin_bit_cast(bf16x2, a), __builtin_bit_cast(bf16x2, b), c, false);
}
template <int CTRL> __device__ __forceinline__ float dpp(float x) {
  return __builtin_bit_cast(float, __builtin_amdgcn_mov_dpp(__builtin_bit_cast(int, x), CTRL, 0xf, 0xf, true));
}
constexpr int XOR1 = 0xB1, XOR2 = 0x4E, HMIR = 0x141, RMIR = 0x140, ROR8 = 0x128;
__device__ __forceinline__ float p16sum(float a, float b) {
  auto s = __builtin_amdgcn_permlane16_swap(__float_as_uint(a), __float_as_uint(b), false, false);
  return __uint_as_float(s[0]) + __uint_as_float(s[1]);
}
__device__ __forceinline__ float p32sum(float a, float b) {
  auto s = __builtin_amdgcn_permlane32_swap(__float_as_uint(a), __float_as_uint(b), false, false);
  return __uint_as_float(s[0]) + __uint_as_float(s[1]);
}
__device__ __forceinline__ float sum8l(float v) { v += dpp<XOR1>(v); v += dpp<XOR2>(v); return v + dpp<HMIR>(v); }
__device__ __forceinline__ float sum16l(float v) { v = sum8l(v); return v + dpp<RMIR>(v); }
__device__ __forceinline__ float half_sum(float v) { v = sum16l(v); return p16sum(v, v); }
__device__ __forceinline__ float max8l(float v) { v = fmaxf(v, dpp<XOR1>(v)); v = fmaxf(v, dpp<XOR2>(v)); return fmaxf(v, dpp<HMIR>(v)); }
__device__ __forceinline__ float wave_max(float v) {
  v = max8l(v); v = fmaxf(v, dpp<RMIR>(v));
  auto s = __builtin_amdgcn_permlane16_swap(__float_as_uint(v), __float_as_uint(v), false, false); v = fmaxf(__uint_as_float(s[0]), __uint_as_float(s[1]));
  auto t = __builtin_amdgcn_permlane32_swap(__float_as_uint(v), __float_as_uint(v), false, false); return fmaxf(__uint_as_float(t[0]), __uint_as_float(t[1]));
}
template <int N> __device__ __forceinline__ float bcast8(float x) {
  int r = __builtin_amdgcn_update_dpp(0, __builtin_bit_cast(int, x), 0x150 + N, 0xf, 0x3, false);
  r = __builtin_amdgcn_update_dpp(r, __builtin_bit_cast(int, x), 0x158 + N, 0xf, 0xc, false);
  return __builtin_bit_cast(float, r);
}
__device__ __forceinline__ float rl(float v, int lane) { return __uint_as_float(__builtin_amdgcn_readlane(__float_as_uint(v), lane)); }
__device__ __forceinline__ float ex2(float v) { return __builtin_amdgcn_exp2f(v); }
__device__ __forceinline__ float silu(float z) { return z * __builtin_amdgcn_rcpf(1.f + __expf(-z)); }
__device__ __forceinline__ float sum8(const float (&v)[8]) { return ((v[0] + v[1]) + (v[2] + v[3])) + ((v[4] + v[5]) + (v[6] + v[7])); }
__device__ __forceinline__ u32 now() { return (u32)__builtin_amdgcn_s_memrealtime(); }
__device__ __forceinline__ void wait_until(u32 t) {
  _Pragma("unroll 1") for (int i = 0; i < 2048 && (int)(now() - t) < 0; i++) __builtin_amdgcn_s_sleep(2);
}
template <int SLEEP> __device__ __forceinline__ void lds_wait(LAS char* p, u32 want) {
  _Pragma("unroll 1") for (u32 i = 0; i < (1u << 22) && *(volatile LAS u32*)p < want; i++) __builtin_amdgcn_s_sleep(SLEEP);
  asm volatile("" ::: "memory");
}
__device__ __forceinline__ void lds_signal(LAS char* p, int lane) {
  asm volatile("s_waitcnt lgkmcnt(0)" ::: "memory");
  if (lane == 0) __hip_atomic_fetch_add((LAS u32*)p, 1u, RLX_WG);
}
__device__ __forceinline__ i32x4 srd(const void* p, u32 bytes) { return i32x4{(int)(size_t)p, (int)((size_t)p >> 32), (int)bytes, 0x00020000}; }

template <int TB> __device__ __forceinline__ u32 enc(float v, u32 tlo) { return ((__float_as_uint(v) + (1u << (TB - 1))) >> TB) | (tlo & (0xffffffffu << (32 - TB))); }
template <int TB> __device__ __forceinline__ u32 miss(u32 lo, u32 hi, u32 thi, u32 tlo) { return (hi ^ thi) | ((lo ^ tlo) >> (32 - TB)); }
template <int TB> __device__ __forceinline__ void put2(rsrc_t rw, u32 gi, float a, float b, u32 thi, u32 tlo) {
  __builtin_amdgcn_raw_buffer_store_b128(u32x4{enc<TB>(a, tlo), thi, enc<TB>(b, tlo), thi}, rw, gi * 8, 0, 16);
}
template <int TB> __device__ __forceinline__ void put_rows(rsrc_t rw, u32 gi, float r0, float r1, int lane, bool on, u32 thi, u32 tlo) {
  const float ex = dpp<XOR1>((lane & 1) ? r0 : r1);
  if (on) put2<TB>(rw, gi, (lane & 1) ? ex : r0, (lane & 1) ? r1 : ex, thi, tlo);
}
__device__ __forceinline__ void put_hint(rsrc_t rw, u32 gi, u32 w24, int lane, u32 thi, u32 tlo) {
  if (lane < 8) __builtin_amdgcn_raw_buffer_store_b128(u32x4{w24 | tlo, thi, w24 | tlo, thi}, rw, (gi + 2 * lane) * 8, 0, 16);
}
template <int TB, int N, int S, int SLEEP> __device__ __forceinline__ void gather(rsrc_t rw, u32 gi, float (&v)[N], u32 thi, u32 tlo) {
  u32x2 x[N];
#pragma unroll
  for (int t = 0; t < N; t++) x[t] = __builtin_amdgcn_raw_buffer_load_b64(rw, gi * 8, t * S * 8, 16);
#pragma unroll 1
  for (u32 spin = 0; spin < SPIN_CAP; spin++) {
    u32 m = 0;
#pragma unroll
    for (int t = 0; t < N; t++) m |= miss<TB>(x[t][0], x[t][1], thi, tlo);
    if (__builtin_amdgcn_ballot_w64(m != 0) == 0) break;
    __builtin_amdgcn_s_sleep(SLEEP);
#pragma unroll
    for (int t = 0; t < N; t++) if (miss<TB>(x[t][0], x[t][1], thi, tlo)) x[t] = __builtin_amdgcn_raw_buffer_load_b64(rw, gi * 8, t * S * 8, 16);
    asm volatile("" ::: "memory");
  }
#pragma unroll
  for (int t = 0; t < N; t++) v[t] = __uint_as_float(x[t][0] << TB);
}
template <int TB, int N, int S, int SLEEP> __device__ __forceinline__ void gather2(rsrc_t rw, u32 gi, float (&a)[N], float (&b)[N], u32 thi, u32 tlo) {
  u32x4 x[N];
#pragma unroll
  for (int t = 0; t < N; t++) x[t] = __builtin_amdgcn_raw_buffer_load_b128(rw, gi * 8, t * S * 8, 16);
#pragma unroll 1
  for (u32 spin = 0; spin < SPIN_CAP; spin++) {
    u32 m = 0;
#pragma unroll
    for (int t = 0; t < N; t++) m |= miss<TB>(x[t][0], x[t][1], thi, tlo) | miss<TB>(x[t][2], x[t][3], thi, tlo);
    if (__builtin_amdgcn_ballot_w64(m != 0) == 0) break;
    __builtin_amdgcn_s_sleep(SLEEP);
#pragma unroll
    for (int t = 0; t < N; t++) if (miss<TB>(x[t][0], x[t][1], thi, tlo) | miss<TB>(x[t][2], x[t][3], thi, tlo)) x[t] = __builtin_amdgcn_raw_buffer_load_b128(rw, gi * 8, t * S * 8, 16);
    asm volatile("" ::: "memory");
  }
#pragma unroll
  for (int t = 0; t < N; t++) { a[t] = __uint_as_float(x[t][0] << TB); b[t] = __uint_as_float(x[t][2] << TB); }
}

#define LDNT(dst, rs, voff, soff) asm volatile("buffer_load_dwordx4 %0, %1, %2, %3 offen nt" : "=v"(dst) : "v"(voff), "s"(rs), "s"(soff))
#define LDP(dst, rs, voff, soff) asm volatile("buffer_load_dwordx4 %0, %1, %2, %3 offen" : "=v"(dst) : "v"(voff), "s"(rs), "s"(soff))
#define BAR() asm volatile("s_waitcnt lgkmcnt(0)\n\ts_barrier" ::: "memory")

__global__ void __launch_bounds__(768, 3)
fused_kernel(const u16* __restrict__ x, const u16* __restrict__ Kc, const u16* __restrict__ Vc, const u16* __restrict__ wq, const u16* __restrict__ wo,
             const u16* __restrict__ wr, const u16* __restrict__ wg, const u16* __restrict__ wu, const u16* __restrict__ wd, u16* __restrict__ out, char* ws) {
  __shared__ __attribute__((aligned(16))) char lds_[L_END];
  LAS char* lds = (LAS char*)lds_;
  const u32 t0 = now();
  u64 pc0; asm volatile("s_getpc_b64 %0" : "=s"(pc0));
  const int blk = blockIdx.x, tid = threadIdx.x, lane = tid & 63, wave = __builtin_amdgcn_readfirstlane(tid >> 6);
  const int hp = 4 * (blk & 7) + (blk >> 6), j = (blk >> 3) & 7, pid = hp * 8 + j;
  asm volatile("" :: "s"(x), "s"(Kc), "s"(Vc), "s"(wq), "s"(wo), "s"(wg), "s"(wu), "s"(wd), "s"(ws));
  if (wave == 4 && lane < 10) *(LAS u32*)(lds + L_CTL + lane * 4) = 0;
  const int g = lane >> 4, i16 = lane & 15;
  const rsrc_t rw = __builtin_amdgcn_make_buffer_rsrc(ws, 0, (int)(IEND * 8), 0x00020000);
  const int fb = blk >= 40, ft = (blk < 80 ? blk : 0) - 40 * fb, fj = ft / 5, fe = fb * D + 16 * ft + i16;
  const u32 gf = I2 + (g * 8 + fj) * S2 + (ft - 5 * fj) * 32 + fb * 16 + i16;
  const u32 ph64 = ((blk >> 6) * 2 + (j >> 2)) * 8 + (blk & 7);

  if (wave >= 4) {
    const int sw = wave - 4, r = lane >> 3, c = lane & 7;
    const i32x4 rk = srd(Kc, KVB), rv = srd(Vc, KVB);
    constexpr u32 ST = 32 * 3072, RB = 8 * ST;
    const u32 so0 = (u32)((j >> 2) * H + hp) * (SK * 384u) + (sw * 4 + (j & 3)) * 3072u;
    const u32 loff = r * 384 + c * 16;
    u32x4 rg[8][3];
    float dd = 0.f;
    f32x2 acc[12];
    u32 tau = TAU, gate = ((t0 + G_OFF) << 4) + ph64 * (TAU / 64) - TAU;
#define PACE_ASM(g) "1:\n\ts_memrealtime vcc\n\ts_waitcnt lgkmcnt(0)\n\ts_lshl_b32 vcc_lo, vcc_lo, 4\n\ts_sub_u32 vcc_lo, vcc_lo, " g "\n\ts_cmp_lt_i32 vcc_lo, 0\n\ts_cbranch_scc0 2f\n\ts_sleep 1\n\ts_branch 1b\n2:\n\t"
#define LOADS3(v, r, s) "s_waitcnt vmcnt(0)\n\tbuffer_load_dwordx4 %0, " v ", " r ", " s " offen nt\n\tbuffer_load_dwordx4 %1, " v ", " r ", " s " offen offset:128 nt\n\tbuffer_load_dwordx4 %2, " v ", " r ", " s " offen offset:256 nt"
#define TLD0(dst, rs, soff) do { gate += tau; asm volatile(PACE_ASM("%6") LOADS3("%3", "%4", "%5") : "=&v"(dst[0]), "=&v"(dst[1]), "=&v"(dst[2]) : "v"(loff), "s"(rs), "s"(soff), "s"(gate) : "vcc", "scc"); } while (0)
#define TLDK(dst, nx, rs, soff) do { gate += tau; asm volatile(PACE_ASM("%9") LOADS3("%6", "%7", "%8") \
    : "=&v"(dst[0]), "=&v"(dst[1]), "=&v"(dst[2]), "+v"(nx[0]), "+v"(nx[1]), "+v"(nx[2]) : "v"(loff), "s"(rs), "s"(soff), "s"(gate), "v"(dd) : "vcc", "scc"); } while (0)
#define TLDV(dst, nx, soff, NLAST) do { gate += tau; asm volatile("s_cmp_eq_u32 %10, 3\n\ts_cbranch_scc1 3f\n\t" PACE_ASM("%9") LOADS3("%6", "%7", "%8") "\n\ts_branch 4f\n3:\n\ts_waitcnt vmcnt(" #NLAST ")\n4:" \
    : "=&v"(dst[0]), "=&v"(dst[1]), "=&v"(dst[2]), "+v"(nx[0]), "+v"(nx[1]), "+v"(nx[2]) : "v"(loff), "s"(rv), "s"(soff), "s"(gate), "s"(it), \
      "v"(acc[0]), "v"(acc[1]), "v"(acc[2]), "v"(acc[3]), "v"(acc[4]), "v"(acc[5]), "v"(acc[6]), "v"(acc[7]), "v"(acc[8]), "v"(acc[9]), "v"(acc[10]), "v"(acc[11]) : "vcc", "scc"); } while (0)
    BAR();
    if (sw == 0 && blk >= 16 && blk < 32) {
      u32 d0, d1, d2;
      const u32 vo = lane * 128;
      asm volatile("global_load_dword %0, %3, %4\n\tglobal_load_dword %1, %5, %4\n\tglobal_load_dword %2, %6, %4\n\ts_waitcnt vmcnt(0)"
                   : "=&v"(d0), "=&v"(d1), "=&v"(d2) : "v"(vo), "s"(pc0), "v"(vo + 8192), "v"(min(vo + 16384, CODE_BYTES - 4u)) : "memory");
    }
    const bool rep = sw == 7 && blk < 16;
#pragma unroll
    for (int s = 0; s < 8; s++) {
      TLD0(rg[s], rk, so0 + s * ST);
      if (s == A_G && rep) {
        lds_wait<1>(lds + C_TAGOK, 1);
        const u32x2 tg = *(const LAS u32x2*)(lds + C_TAG);
        put_hint(rw, IH + blk * 16, now() & 0xffffffu, lane, tg[0], tg[1]);
      }
    }
    BAR();
    asm volatile("s_waitcnt vmcnt(21)" : "+v"(rg[0][0]), "+v"(rg[0][1]), "+v"(rg[0][2]));
    gate = ((u32)__builtin_amdgcn_readfirstlane(*(const LAS u32*)(lds + C_ANCHOR)) << 4) + ph64 * (TAU / 64) - TAU;
    u32 lag1 = 0;
#define LAGNOW() (((now() << 4) - gate) >> 4)
#define FIRST(PH) { lag1 = min(LAGNOW(), 2047u); if (lane == 0) *(volatile LAS u32*)(lds + C_FIRST + (PH) * 4) = lag1 + 1; }
#define SAMPLE(PH) { const u32 l2 = min(LAGNOW(), 2047u); if (lane == 0) *(volatile LAS u32*)(lds + C_SAMP + (PH) * 4) = 0x800000u | (lag1 << 11) | l2; }
#define DECIDE(PH) { u32 w_ = 0; _Pragma("unroll 1") for (int i_ = 0; i_ < 8; i_++) { w_ = __builtin_amdgcn_readfirstlane(*(volatile LAS u32*)(lds + C_DEC + (PH) * 4)); if (w_) break; __builtin_amdgcn_s_sleep(2); } \
      tau += (w_ >> 16) & 0x7fffu; gate += (w_ & 0xffffu) << 4; }
    float t0_ = 0.f, t1_ = 0.f, t2_ = 0.f, t3_ = 0.f;
    u32x4 qf[3];
#pragma unroll
    for (int t = 0; t < 3; t++) qf[t] = *(LAS u32x4*)(lds + L_Q + 128 * t + 16 * c);
#pragma unroll 1
    for (int it = 0; it < 4; it++) {
      const i32x4 rn = it < 3 ? rk : rv;
      const u32 sn = so0 + ((it + 1) & 3) * RB;
      if (it == 3) tau = TAUV;
      float col = 0.f;
#pragma unroll
      for (int s = 0; s < 8; s++) {
        float d0 = 0.f, d1 = 0.f, d2 = 0.f;
#pragma unroll
        for (int e = 0; e < 4; e++) { d0 = dot2(rg[s][0][e], qf[0][e], d0); d1 = dot2(rg[s][1][e], qf[1][e], d1); d2 = dot2(rg[s][2][e], qf[2][e], d2); }
        dd = d0 + d1 + d2;
        if (s == 7 && it == 1) DECIDE(0)
        TLDK(rg[s], rg[(s + 1) & 7], rn, sn + s * ST);
        if (s == 3 && rep) { if (it == 3) FIRST(1) if (it == 1) SAMPLE(0) }
        if (s == 7 && rep) { if (it == 0) FIRST(0) if (it == 3) SAMPLE(1) }
        dd = sum8l(dd);
        col = c == s ? dd : col;
      }
      t0_ = t1_; t1_ = t2_; t2_ = t3_;
      t3_ = rbf(rbf(col) * 0.07216878364870322f) * 1.4426950408889634f;
    }
    { const float M = wave_max(fmaxf(fmaxf(t0_, t1_), fmaxf(t2_, t3_)));
      t0_ = ex2(t0_ - M); t1_ = ex2(t1_ - M); t2_ = ex2(t2_ - M); t3_ = ex2(t3_ - M);
      float l = half_sum((t0_ + t1_) + (t2_ + t3_)); l = p32sum(l, l);
      if (lane == 0) *(LAS f32x2*)(lds + L_ML + sw * 8) = f32x2{M, l};
      lds_signal(lds + C_ML, lane); }
#pragma unroll
    for (int t = 0; t < 12; t++) acc[t] = f32x2{0.f, 0.f};
    const int ai0 = 3 * ((lane >> 3) & 1) + 6 * ((lane >> 4) & 1) + 12 * (lane >> 5);
    u32 pa[3];
#pragma unroll
    for (int e = 0; e < 3; e++) pa[e] = L_PART + (sw * 192 + 64 * ((ai0 + e) >> 3) + 8 * c + ((ai0 + e) & 7)) * 4;
    asm volatile("" : "+v"(pa[0]), "+v"(pa[1]), "+v"(pa[2]));
#define PV(S) { const float p = bcast8<S>(t0_); \
      _Pragma("unroll") for (int t = 0; t < 3; t++) _Pragma("unroll") for (int e = 0; e < 4; e++) { \
        const u32 w_ = rg[S][t][e]; acc[t * 4 + e] += p * f32x2{lo16(w_), hi16(w_)}; } }
#define PVL(S, NLAST) PV(S) TLDV(rg[S], rg[(S + 1) & 7], sn + S * ST, NLAST);
#pragma unroll 1
    for (int it = 0; it < 4; it++) {
      const u32 sn = so0 + (it + 1) * RB;
      PVL(0, 18) PVL(1, 15) PVL(2, 12) PV(3)
      if (it == 0) DECIDE(1)
      TLDV(rg[3], rg[4], sn + 3 * ST, 9);
      PVL(4, 6) PVL(5, 3)
      if (it == 2 && sw < 4) tau -= ADV * 16;
      PVL(6, 0) PVL(7, 0)
      t0_ = t1_; t1_ = t2_; t2_ = t3_;
      if (it == 2 && sw == 7 && lane == 0) *(volatile LAS u32*)(lds + C_LATE) = 1;
    }
    {
      float a12[12], a6[6];
#pragma unroll
      for (int e = 0; e < 12; e++) a12[e] = p32sum(acc[e >> 1][e & 1], acc[6 + (e >> 1)][e & 1]);
#pragma unroll
      for (int e = 0; e < 6; e++) a6[e] = p16sum(a12[e], a12[e + 6]);
      const bool b3 = lane & 8;
#pragma unroll
      for (int e = 0; e < 3; e++) {
        const float keep = b3 ? a6[e + 3] : a6[e], send = b3 ? a6[e] : a6[e + 3];
        *(LAS float*)(lds + pa[e]) = keep + dpp<ROR8>(send);
      } }
    const u32x2 tg = *(const LAS u32x2*)(lds + C_TAG);
    BAR();
    if (sw < 6) {
      const int pb = sw >= 3, pd = 64 * (sw - 3 * pb) + lane;
      __builtin_amdgcn_s_sleep(G1_DELAY);
      if (pb > (j >> 2)) __builtin_amdgcn_s_sleep(4);
      float v[4];
      gather<TO, 4, S1, PSLEEP>(rw, I1 + (hp * 8 + 4 * pb) * S1 + pd, v, tg[0], tg[1]);
      *(LAS u16*)(lds + L_O + pb * 384 + pd * 2) = (u16)f2bf((v[0] + v[1]) + (v[2] + v[3]));
    }
    BAR();
    if (blk < 80 && sw == 7) {
      const f32x2 xg = *(const LAS f32x2*)(lds + L_FIN + i16 * 8);
      wait_until(now() + (((63 - ph64) * (TAU / 64)) >> 4));
      __builtin_amdgcn_s_sleep(G2_DELAY);
      float v[8];
      gather<TO, 8, 4 * 8 * S2, PSLEEP>(rw, gf, v, tg[0], tg[1]);
      float S = sum8(v); S = p16sum(S, S); S = p32sum(S, S);
      const u32 res = f2bf(xg[0] + rbf(rbf(S) * xg[1])), nb = __builtin_bit_cast(u32, dpp<XOR1>(__builtin_bit_cast(float, res)));
      if (lane < 16 && !(lane & 1)) *(GAS u32*)(out + fe) = res | (nb << 16);
      if (blk == 0 && lane == 0) __hip_atomic_store((GAS u64*)ws, *(const LAS u64*)(lds + C_EPOCH) + 1, RLX_AGENT);
    }
  } else {
    const int q4 = i16 >> 2, p4 = lane & 3, l32 = lane & 31, hb = lane >> 5;
    u32x2 xs[5], xa[5], ep;
    float mb, rd, mb0, mb1, rd0, rd1;
    s16x4 af[5];
    u32 thi, tlo;
    const u32 l10 = lane / 10, vb = lane < 60 ? l10 * (D * 2) + (lane - 10 * l10) * 16 : 0x7ffffff0u;
    const u32 sbo = (DH * hp + 64 * wave) * (D * 2) + 160 * j;
#define LOAD_X() { \
      const i32x4 rx = srd(x, 2 * D * 2); \
      const u32 o1 = hb * 1280 + 8 * l32, o2 = (lane & 1) * 1280 + 8 * g, zero = 0; \
      asm volatile("buffer_load_dwordx2 %0, %11, %13, 0 offen\n\tbuffer_load_dwordx2 %1, %11, %13, 0 offen offset:256\n\tbuffer_load_dwordx2 %2, %11, %13, 0 offen offset:512\n\t" \
                   "buffer_load_dwordx2 %3, %11, %13, 0 offen offset:768\n\tbuffer_load_dwordx2 %4, %11, %13, 0 offen offset:1024\n\t" \
                   "buffer_load_dwordx2 %5, %12, %13, %14 offen\n\tbuffer_load_dwordx2 %6, %12, %13, %14 offen offset:32\n\tbuffer_load_dwordx2 %7, %12, %13, %14 offen offset:64\n\t" \
                   "buffer_load_dwordx2 %8, %12, %13, %14 offen offset:96\n\tbuffer_load_dwordx2 %9, %12, %13, %14 offen offset:128\n\t" \
                   "global_load_dwordx2 %10, %15, %16 sc1" \
                   : "=&v"(xs[0]), "=&v"(xs[1]), "=&v"(xs[2]), "=&v"(xs[3]), "=&v"(xs[4]), "=&v"(xa[0]), "=&v"(xa[1]), "=&v"(xa[2]), "=&v"(xa[3]), "=&v"(xa[4]), "=&v"(ep) \
                   : "v"(o1), "v"(o2), "s"(rx), "s"(160 * j), "v"(zero), "s"(ws) : "memory"); }
#define LAYERNORM(N) { \
      asm volatile("s_barrier\n\ts_waitcnt vmcnt(" #N ")" : "+v"(xs[0]), "+v"(xs[1]), "+v"(xs[2]), "+v"(xs[3]), "+v"(xs[4]), "+v"(xa[0]), "+v"(xa[1]), "+v"(xa[2]), "+v"(xa[3]), "+v"(xa[4]), "+v"(ep) :: "memory"); \
      float s = 0.f, ss = 0.f; \
      _Pragma("unroll") for (int t = 0; t < 5; t++) s += (lo16(xs[t][0]) + hi16(xs[t][0])) + (lo16(xs[t][1]) + hi16(xs[t][1])); \
      const float mean = half_sum(s) * (1.f / D); \
      _Pragma("unroll") for (int t = 0; t < 5; t++) { const float d0 = lo16(xs[t][0]) - mean, d1 = hi16(xs[t][0]) - mean, d2 = lo16(xs[t][1]) - mean, d3 = hi16(xs[t][1]) - mean; ss += (d0 * d0 + d1 * d1) + (d2 * d2 + d3 * d3); } \
      const float var = rbf(half_sum(ss) * (1.f / D)); mb = rbf(mean); \
      rd = __builtin_amdgcn_rcpf(rbf(__builtin_amdgcn_sqrtf(rbf(var + 1e-5f)))); \
      mb0 = rl(mb, 0); mb1 = rl(mb, 32); rd0 = rl(rd, 0); rd1 = rl(rd, 32); \
      const float mba = (lane & 1) ? mb1 : mb0, rda = (lane & 1) ? rd1 : rd0; \
      _Pragma("unroll") for (int t = 0; t < 5; t++) { \
        u32x2 o; o[0] = pack2(rbf(lo16(xa[t][0]) - mba) * rda, rbf(hi16(xa[t][0]) - mba) * rda); o[1] = pack2(rbf(lo16(xa[t][1]) - mba) * rda, rbf(hi16(xa[t][1]) - mba) * rda); \
        af[t] = __builtin_bit_cast(s16x4, o); } \
        \
      const u64 tg = (((u64)ep[1] << 32) | ep[0]) * 0x9E3779B97F4A7C15ull + 0x632BE59BD9B4E019ull; thi = (u32)tg; tlo = (u32)(tg >> 32) << 24; }
#define WAIT11(w, N) asm volatile("s_waitcnt vmcnt(" #N ")" : "+v"(w[0]), "+v"(w[1]), "+v"(w[2]), "+v"(w[3]), "+v"(w[4]), "+v"(w[5]), "+v"(w[6]), "+v"(w[7]), "+v"(w[8]), "+v"(w[9]), "+v"(w[10]) :: "memory")
#define STAGE_WO(w) if (lane < 60) { _Pragma("unroll") for (int t = 0; t < 11; t++) if (t < 10 || lane < 40) *(LAS u32x4*)(lds + L_WO + (64 * wave + 6 * t) * 160 + lane * 16) = w[t]; }
    if (wave < 3) {
      u32x4 wa[10], wb[11];
      LOAD_X()
      { const i32x4 ra = srd(wq, D * H * DH * 2), rb = srd(wo, wave == 0 ? D * H * DH * 2 : 0);
        const u32 va = (lane >> 3) * (H * DH * 2) + (lane & 7) * 16, sa = 80 * j * (H * DH * 2) + (hp * DH + 64 * wave) * 2;
#pragma unroll
        for (int t = 0; t < 10; t++) LDNT(wa[t], ra, va, sa + t * 8 * (H * DH * 2));
#pragma unroll
        for (int t = 0; t < 11; t++) LDP(wb[t], rb, vb, sbo + t * 6 * D * 2); }
      LAYERNORM(21)
      asm volatile("s_waitcnt vmcnt(11)" : "+v"(wa[0]), "+v"(wa[1]), "+v"(wa[2]), "+v"(wa[3]), "+v"(wa[4]), "+v"(wa[5]), "+v"(wa[6]), "+v"(wa[7]), "+v"(wa[8]), "+v"(wa[9]),
                   "+v"(af[0]), "+v"(af[1]), "+v"(af[2]), "+v"(af[3]), "+v"(af[4]));
      LAS char* wl = lds + L_WQ + wave * (80 * 160);
#pragma unroll
      for (int t = 0; t < 10; t++) *(LAS u32x4*)(wl + (8 * t + (lane >> 3)) * 160 + (lane & 7) * 16) = wa[t];
      f32x4 ac[4];
#pragma unroll
      for (int t = 0; t < 4; t++) {
        ac[t] = f32x4{0.f, 0.f, 0.f, 0.f};
#pragma unroll
        for (int ks = 0; ks < 5; ks++) {
          const s16x4 bf = __builtin_amdgcn_ds_read_tr16_b64_v4i16((LAS s16x4*)(wl + (16 * ks + 4 * g + q4) * 160 + (16 * t + 4 * p4) * 2));
          ac[t] = __builtin_amdgcn_mfma_f32_16x16x16bf16_1k(af[ks], bf, ac[t], 0, 0, 0);
        }
      }
      put_rows<TQ>(rw, I0 + pid * S0 + (lane & 1) * 192 + 64 * wave + (lane & ~1), g == 0 ? ac[0][0] : g == 1 ? ac[1][0] : g == 2 ? ac[2][0] : ac[3][0],
                   g == 0 ? ac[0][1] : g == 1 ? ac[1][1] : g == 2 ? ac[2][1] : ac[3][1], lane, true, thi, tlo);
      WAIT11(wb, 1);
      STAGE_WO(wb)
      wait_until(t0 + T_Q);
      float v[8];
      gather<TQ, 8, S0, 8>(rw, I0 + hp * 8 * S0 + (j >> 2) * 192 + 64 * wave + lane, v, thi, tlo);
      *(LAS u16*)(lds + L_Q + (64 * wave + lane) * 2) = (u16)f2bf(sum8(v));
      if (wave == 2) {
        wait_until(t0 + A_T);
        const u32x2 h = __builtin_amdgcn_raw_buffer_load_b64(rw, (IH + i16 * 16) * 8, 0, 16);
        const int rel = miss<TO>(h[0], h[1], thi, tlo) ? (int)(now() - t0) : (int)((h[0] - t0) << 8) >> 8;
        const int ts = min(max((int)(sum16l((float)rel) * (1.f / 16)) + A_C, T_LO), T_HI);
        if (lane == 0) *(LAS u32*)(lds + C_ANCHOR) = t0 + ts;
      }
    } else {
      u32x4 wa[16], wb[8];
      LOAD_X()
      { const i32x4 r1 = srd(wg, D * F * 2), r2 = srd(wu, D * F * 2), rb = srd(wd, D * F * 2);
        const u32 l6 = lane / 6, va = lane < 60 ? l6 * (F * 2) + (lane - 6 * l6) * 16 : 0x7ffffff0u, sa = 80 * j * (F * 2) + hp * 96, sb = 48 * hp * (D * 2) + 160 * j;
#pragma unroll
        for (int t = 0; t < 8; t++) LDP(wa[t], r1, va, sa + t * 10 * F * 2);
#pragma unroll
        for (int t = 0; t < 8; t++) LDP(wa[8 + t], r2, va, sa + t * 10 * F * 2);
#pragma unroll
        for (int t = 0; t < 8; t++) LDP(wb[t], rb, vb, sb + t * 6 * D * 2); }
      LAYERNORM(24)
      if (lane == 0) { *(LAS u32x2*)(lds + C_TAG) = u32x2{thi, tlo}; *(LAS u32x2*)(lds + C_EPOCH) = ep; }
      lds_signal(lds + C_TAGOK, lane);
      asm volatile("s_waitcnt vmcnt(8)" : "+v"(wa[0]), "+v"(wa[1]), "+v"(wa[2]), "+v"(wa[3]), "+v"(wa[4]), "+v"(wa[5]), "+v"(wa[6]), "+v"(wa[7]), "+v"(wa[8]), "+v"(wa[9]),
                   "+v"(wa[10]), "+v"(wa[11]), "+v"(wa[12]), "+v"(wa[13]), "+v"(wa[14]), "+v"(wa[15]), "+v"(af[0]), "+v"(af[1]), "+v"(af[2]), "+v"(af[3]), "+v"(af[4]));
      LAS char* wl = lds + L_GU;
      if (lane < 60) {
#pragma unroll
        for (int t = 0; t < 16; t++) *(LAS u32x4*)(wl + t * 960 + lane * 16) = wa[t];
      }
      f32x4 ac[6];
#pragma unroll
      for (int t = 0; t < 6; t++) {
        ac[t] = f32x4{0.f, 0.f, 0.f, 0.f};
#pragma unroll
        for (int ks = 0; ks < 5; ks++) {
          const s16x4 bf = __builtin_amdgcn_ds_read_tr16_b64_v4i16((LAS s16x4*)(wl + (t / 3) * 7680 + (16 * ks + 4 * g + q4) * 96 + (16 * (t % 3) + 4 * p4) * 2));
          ac[t] = __builtin_amdgcn_mfma_f32_16x16x16bf16_1k(af[ks], bf, ac[t], 0, 0, 0);
        }
      }
      if (g < 3) {
        const u32 gi = I0 + pid * S0 + 384 + 2 * lane;
        put2<TQ>(rw, gi, g == 0 ? ac[0][0] : g == 1 ? ac[1][0] : ac[2][0], g == 0 ? ac[3][0] : g == 1 ? ac[4][0] : ac[5][0], thi, tlo);
        put2<TQ>(rw, gi + 96, g == 0 ? ac[0][1] : g == 1 ? ac[1][1] : ac[2][1], g == 0 ? ac[3][1] : g == 1 ? ac[4][1] : ac[5][1], thi, tlo);
      }
      asm volatile("s_waitcnt vmcnt(0)" : "+v"(wb[0]), "+v"(wb[1]), "+v"(wb[2]), "+v"(wb[3]), "+v"(wb[4]), "+v"(wb[5]), "+v"(wb[6]), "+v"(wb[7]) :: "memory");
      if (lane < 60) {
#pragma unroll
        for (int t = 0; t < 8; t++) *(LAS u32x4*)(lds + L_WO + (200 + 6 * t) * 160 + lane * 16) = wb[t];
      }
      wait_until(t0 + T_H);
#pragma unroll 1
      for (int b = 0; b < 2; b++) {
        float gs[8], us[8];
        gather2<TQ, 8, S0, 16>(rw, I0 + hp * 8 * S0 + 384 + b * 96 + 2 * (lane < 48 ? lane : 0), gs, us, thi, tlo);
        if (lane < 48) *(LAS u16*)(lds + L_H + b * 128 + lane * 2) = (u16)f2bf(rbf(silu(rbf(sum8(gs)))) * rbf(sum8(us)));
      }
      f32x4 y[5];
#pragma unroll
      for (int tl = 0; tl < 5; tl++) {
        y[tl] = f32x4{0.f, 0.f, 0.f, 0.f};
#pragma unroll
        for (int ks = 0; ks < 3; ks++) {
          const s16x4 a = *(const LAS s16x4*)(lds + L_H + (lane & 1) * 128 + (16 * ks + 4 * g) * 2);
          const s16x4 bf = __builtin_amdgcn_ds_read_tr16_b64_v4i16((LAS s16x4*)(lds + L_WO + (16 * tl + 4 * p4) * 2 + (200 + 16 * ks + 4 * g + q4) * 160));
          y[tl] = __builtin_amdgcn_mfma_f32_16x16x16bf16_1k(a, bf, y[tl], 0, 0, 0);
        }
      }
      const u32 gi = I2 + pid * S2 + 160 + (lane & 1) * 16 + (i16 & ~1);
      put_rows<TO>(rw, gi + g * 32, g == 0 ? y[0][0] : g == 1 ? y[1][0] : g == 2 ? y[2][0] : y[3][0], g == 0 ? y[0][1] : g == 1 ? y[1][1] : g == 2 ? y[2][1] : y[3][1], lane, true, thi, tlo);
      put_rows<TO>(rw, gi + 128, y[4][0], y[4][1], lane, g == 0, thi, tlo);
    }
    BAR();
    if (wave == 0 && blk < 80) {
      float rs = 0.f;
#pragma unroll
      for (int t = 0; t < 5; t++) {
        const u32x2 w = *(const GAS u32x2*)((const GAS char*)wr + 8 * (l32 + 32 * t)), xw = *(const GAS u32x2*)((const GAS char*)x + hb * 1280 + 8 * (l32 + 32 * t));
        rs += rbf(rbf(lo16(xw[0]) - mb) * rd) * lo16(w[0]) + rbf(rbf(hi16(xw[0]) - mb) * rd) * hi16(w[0]) + rbf(rbf(lo16(xw[1]) - mb) * rd) * lo16(w[1]) + rbf(rbf(hi16(xw[1]) - mb) * rd) * hi16(w[1]);
      }
      rs = rbf(half_sum(rs));
      rs = rbf(__builtin_amdgcn_rcpf(1.f + __expf(-rs)));
      const float rsb = fb ? rl(rs, 32) : rl(rs, 0);
      const float fx = bf2f(x[fe]), fxn = rbf(rbf(fx - (fb ? mb1 : mb0)) * (fb ? rd1 : rd0));
      wait_until(t0 + T_Y);
      float v[8];
      gather<TO, 8, 4 * 8 * S2, 32>(rw, gf + 160, v, thi, tlo);
      float Y = sum8(v); Y = p16sum(Y, Y); Y = p32sum(Y, Y);
      if (lane < 16) *(LAS f32x2*)(lds + L_FIN + lane * 8) = f32x2{fx, rbf(silu(rbf(fxn + rbf(rbf(Y) * rsb))))};
    }
    if (wave == 1) {
      lds_wait<8>(lds + C_ML, 8);
      const f32x2 ml = *(const LAS f32x2*)(lds + L_ML + (lane & 7) * 8);
      const float M = max8l(ml[0]), e = M + __builtin_amdgcn_logf(sum8l(ml[1] * ex2(ml[0] - M)));
      if (lane < 8) put2<TQ>(rw, I1 + pid * S1 + 192 + 2 * lane, e, e, thi, tlo);
      wait_until(now() + T_LSE);
      float a[1];
      gather<TQ, 1, 0, 64>(rw, I1 + ((pid & ~3) + (lane & 3)) * S1 + 192, a, thi, tlo);
      float m = fmaxf(a[0], dpp<XOR1>(a[0])); m = fmaxf(m, dpp<XOR2>(m));
      float L = ex2(a[0] - m); L += dpp<XOR1>(L); L += dpp<XOR2>(L);
      if (lane < 8) *(LAS float*)(lds + L_FW + lane * 4) = ex2(ml[0] - m) * __builtin_amdgcn_rcpf(L);
    }
    if (wave == 2) {
      u32 ts = *(const LAS u32*)(lds + C_ANCHOR) + ((12 * TAU) >> 4);
#pragma unroll 1
      for (int ph = 0; ph < 2; ph++) {
        const u32 gi = IH + 256 * (ph + 1);
        if (blk < 16) {
          u32 w = 0;
          _Pragma("unroll 1") for (int i = 0; i < 4096 && !w && (int)(now() - (ts + R_T - R_M)) < 0; i++) { w = *(volatile LAS u32*)(lds + C_SAMP + ph * 4); __builtin_amdgcn_s_sleep(1); }
          if (!w) {
            const u32 f = *(volatile LAS u32*)(lds + C_FIRST + ph * 4), a1 = f ? f - 1 : 400u;
            w = 0xc00000u | (a1 << 11) | max(a1, (TAU >> 4) + R_T - R_M);
          }
          put_hint(rw, gi + blk * 16, w, lane, thi, tlo);
        }
        wait_until(ts + R_T);
        const u32x2 h = __builtin_amdgcn_raw_buffer_load_b64(rw, (gi + i16 * 16) * 8, 0, 16);
        const bool ok = !miss<TO>(h[0], h[1], thi, tlo);
        const float n = sum16l(ok ? 1.f : 0.f), l1 = sum16l(ok ? (float)((h[0] >> 11) & 2047u) : 0.f), l2 = sum16l(ok ? (float)(h[0] & 2047u) : 0.f), cn = sum16l(ok ? (float)((h[0] >> 22) & 1u) : 0.f);
        const float rn = __builtin_amdgcn_rcpf(fmaxf(n, 1.f)), sl = (l2 - l1) * rn, lg = fmaxf(l2 * rn - 13.f, 0.f), la = lg + cn * rn * R_CENS;
        const bool slow = n > 3.5f && sl > (ph ? R_SV : R_SK) && sl < R_SMAX && lg > (ph ? R_LV : R_LK);
        const u32 dt = slow ? TAU_SLOW - (ph ? TAUV : TAU) : 0u, sh = (u32)min(slow ? (int)((la + sl) * (ph ? R_SHV : R_SHK)) : la > R_STALL ? (int)(la * 1.1f) : 0, 300);
        if (lane == 0) *(volatile LAS u32*)(lds + C_DEC + ph * 4) = 0x80000000u | (dt << 16) | sh;
        ts += sh + ((12 * TAU + 9 * dt + 8 * TAUV) >> 4);
      }
    }
    u32x4 wl_[11];
    { const i32x4 rb = srd(wo, wave == 1 || wave == 2 ? D * H * DH * 2 : 0);
      asm volatile("s_waitcnt vmcnt(0)" ::: "memory");
      lds_wait<1>(lds + C_LATE, 1);
      wait_until(now() + (((63 - ph64) * (TAU / 64)) >> 4) + T_LATE);
#pragma unroll
      for (int t = 0; t < 11; t++) LDP(wl_[t], rb, vb, sbo + t * 6 * D * 2); }
    BAR();
    if (wave < 3) {
      const int d = 64 * wave + lane;
      const LAS float* part = (const LAS float*)(lds + L_PART) + d;
      const f32x4 f0 = *(const LAS f32x4*)(lds + L_FW), f1 = *(const LAS f32x4*)(lds + L_FW + 16);
      const float o = ((f0[0] * part[0] + f0[1] * part[192]) + (f0[2] * part[384] + f0[3] * part[576])) + ((f1[0] * part[768] + f1[1] * part[960]) + (f1[2] * part[1152] + f1[3] * part[1344]));
      const float ox = dpp<XOR1>(o);
      if (!(lane & 1)) put2<TO>(rw, I1 + pid * S1 + d, o, ox, thi, tlo);
    } else asm volatile("s_waitcnt vmcnt(0)" ::: "memory");
    WAIT11(wl_, 1);
    if (wave == 1 || wave == 2) { STAGE_WO(wl_) lds_signal(lds + C_WO, lane); }
    lds_wait<0>(lds + C_WO, 2);
    s16x8 bw[2][6];
    { const LAS char* wl = lds + L_WO + (16 * wave + 4 * p4) * 2 + (8 * g + q4) * 160;
#pragma unroll
      for (int u = 0; u < 2; u++)
#pragma unroll
        for (int ks = 0; ks < 6; ks++) {
          const LAS char* a = wl + (u ? (4 - wave) * 32 : 0) + 32 * ks * 160;
          const s16x4 b0 = __builtin_amdgcn_ds_read_tr16_b64_v4i16((LAS s16x4*)a), b1 = __builtin_amdgcn_ds_read_tr16_b64_v4i16((LAS s16x4*)(a + 4 * 160));
          bw[u][ks] = __builtin_shufflevector(b0, b1, 0, 1, 2, 3, 4, 5, 6, 7);
        } }
    BAR();
    f32x4 s0 = {0.f, 0.f, 0.f, 0.f}, s1 = {0.f, 0.f, 0.f, 0.f};
#pragma unroll
    for (int ks = 0; ks < 6; ks++) {
      const bf16x8 a = __builtin_bit_cast(bf16x8, *(const LAS s16x8*)(lds + L_O + (lane & 1) * 384 + 64 * ks + 16 * g));
      s0 = __builtin_amdgcn_mfma_f32_16x16x32_bf16(a, __builtin_bit_cast(bf16x8, bw[0][ks]), s0, 0, 0, 0);
      s1 = __builtin_amdgcn_mfma_f32_16x16x32_bf16(a, __builtin_bit_cast(bf16x8, bw[1][ks]), s1, 0, 0, 0);
    }
    put_rows<TO>(rw, I2 + pid * S2 + (wave + g) * 32 + (lane & 1) * 16 + (i16 & ~1), g ? s1[0] : s0[0], g ? s1[1] : s0[1], lane, g == 0 || (g == 1 && wave == 3), thi, tlo);
  }
}

extern "C" void kernel_launch(void* const* d_in, const int* in_sizes, int n_in, void* d_out, int out_size, void* d_ws, size_t ws_size, hipStream_t stream) {
  fused_kernel<<<256, 768, 0, stream>>>((const u16*)d_in[0], (const u16*)d_in[1], (const u16*)d_in[2], (const u16*)d_in[3], (const u16*)d_in[4],
                                        (const u16*)d_in[6], (const u16*)d_in[7], (const u16*)d_in[8], (const u16*)d_in[9], (u16*)d_out, (char*)d_ws);
}
```
